```python
import torch

def _softmax(x, axis=-1):
    return torch.softmax(x, dim=axis)

def _gelu(x):
    return torch.nn.functional.gelu(x, approximate='tanh')

def _silu(x):
    return torch.nn.functional.silu(x)

def _sigmoid(x):
    return torch.sigmoid(x)

def _layernorm(x, axis=-1, eps=1e-05):
    m = x.mean(dim=axis, keepdim=True)
    v = x.var(dim=axis, keepdim=True, unbiased=False)
    return (x - m) / torch.sqrt(v + eps)

def _scatter_add(tgt, idx, src, axis=0):
    return tgt.clone().index_add_(axis, idx, src)

def _segment_sum(src, seg, n):
    out = torch.zeros((n,) + src.shape[1:], dtype=src.dtype)
    return out.index_add_(0, seg, src)

def _pad(x, pw):
    flat = tuple((v for pair in reversed(pw) for v in pair))
    return torch.nn.functional.pad(x, flat)
kernel_name = 'gated_mix__ssm_conv_s4__mixer_gmlp__prompt'

def setup_inputs():

    _w = lambda a: (lambda t: t.to(torch.bfloat16) if t.is_floating_point() else t)(torch.as_tensor(a))

    def _A_setup():
        B, L, D, N = (1, 2048, 640, 192)
        A_re = -(lambda _x: torch.exp(torch.as_tensor(_x)))((lambda *s: torch.randn(*s, dtype=torch.bfloat16) * 3.2)(D, N))
        A_im = torch.pi * (lambda *s: torch.randn(*s, dtype=torch.bfloat16) * 0.1)(D, N)
        B_re = (lambda *s: torch.randn(*s, dtype=torch.bfloat16) * 0.8)(D, N)
        B_im = (lambda *s: torch.randn(*s, dtype=torch.bfloat16) * 0.8)(D, N)
        C_re = (lambda *s: torch.randn(*s, dtype=torch.bfloat16) * 0.8)(D, N)
        C_im = (lambda *s: torch.randn(*s, dtype=torch.bfloat16) * 0.8)(D, N)
        log_dt = (lambda *s: torch.randn(*s, dtype=torch.bfloat16) * 0.1)(D) * 0.5 - 4.0
        Dskip = (lambda *s: torch.randn(*s, dtype=torch.bfloat16) * 0.8)(D)
        return {'u': _w((lambda *s: torch.randn(*s, dtype=torch.bfloat16) * 0.05)(B, L, D)), 'A_re': _w(A_re), 'A_im': _w(A_im), 'B_re': _w(B_re), 'B_im': _w(B_im), 'C_re': _w(C_re), 'C_im': _w(C_im), 'log_dt': _w(log_dt), 'Dskip': _w(Dskip)}
    _A_in = _A_setup()

    def _B_setup():
        B, S, D, F = (1, 2048, 640, 1536)
        Fh = F // 2
        return {'x': _w((lambda *s: torch.randn(*s, dtype=torch.bfloat16) * 0.05)(B, S, D)), 'w_in': _w((lambda *s: torch.randn(*s, dtype=torch.bfloat16) * 0.4)(D, 2 * Fh)), 'w_sp': _w(0.01 * (lambda *s: torch.randn(*s, dtype=torch.bfloat16) * 0.4)(S, S)), 'b_sp': _w(1.0 + 0.01 * (lambda *s: torch.randn(*s, dtype=torch.bfloat16) * 0.05)(S)), 'w_out': _w((lambda *s: torch.randn(*s, dtype=torch.bfloat16) * 0.05)(Fh, D))}
    _B_in = _B_setup()
    return {'A_u': _A_in['u'], 'A_A_re': _A_in['A_re'], 'A_A_im': _A_in['A_im'], 'A_B_re': _A_in['B_re'], 'A_B_im': _A_in['B_im'], 'A_C_re': _A_in['C_re'], 'A_C_im': _A_in['C_im'], 'A_log_dt': _A_in['log_dt'], 'A_Dskip': _A_in['Dskip'], 'B_x': _B_in['x'], 'B_w_in': _B_in['w_in'], 'B_w_sp': _B_in['w_sp'], 'B_b_sp': _B_in['b_sp'], 'B_w_out': _B_in['w_out'], 'gate_w': _w((lambda *s: torch.randn(*s, dtype=torch.bfloat16) * 0.05)(640) * 16.0)}

def reference(A_u, A_A_re, A_A_im, A_B_re, A_B_im, A_C_re, A_C_im, A_log_dt, A_Dskip, B_x, B_w_in, B_w_sp, B_b_sp, B_w_out, gate_w):

    def _A_ref(u, A_re, A_im, B_re, B_im, C_re, C_im, log_dt, Dskip):
        Bsz, L, D = u.shape
        dt = torch.exp(log_dt)[:, None]
        A = A_re + 1j * A_im
        Bc = B_re + 1j * B_im
        Cc = C_re + 1j * C_im
        logdA = dt * A
        dB = dt * Bc
        l = (lambda x, dt: x.to(dt))(torch.arange(L), A_re.dtype)
        P = torch.exp(logdA[:, :, None] * l[None, None, :])
        K = torch.einsum('dn,dnl,dn->dl', Cc, P, dB).real
        L2 = 2 * L
        Kp = _pad(K, ((0, 0), (0, L)))
        up = (lambda x, *d: x.permute(*d))(u, 0, 2, 1)
        up = _pad(up, ((0, 0), (0, 0), (0, L)))
        Kf = (lambda x, axes=(-2, -1): torch.fft.rfft2(x.float(), dim=axes))(Kp[:, None, :], axes=(-2, -1))
        Uf = (lambda x, axes=(-2, -1): torch.fft.rfft2(x.float(), dim=axes))(up[:, :, None, :], axes=(-2, -1))
        Yf = Uf * Kf[None, :, :, :]
        y = (lambda x, s, axes=(-2, -1): torch.fft.irfft2(x, s=s, dim=axes).to(torch.bfloat16))(Yf, (1, L2), axes=(-2, -1))[:, :, 0, :L]
        y = (lambda x, *d: x.permute(*d))(y, 0, 2, 1) + u * Dskip[None, None, :]
        return y

    def _B_ref(x, w_in, w_sp, b_sp, w_out):
        B, S, D = x.shape
        F = w_in.shape[-1]
        Fh = F // 2
        z = _gelu(torch.einsum('bsd,df->bsf', x, w_in))
        z1 = z[..., :Fh].clone()
        z2 = _layernorm(z[..., Fh:], axis=-1)
        g = torch.einsum('ts,bsf->btf', w_sp, z2) + b_sp[None, :, None]
        return torch.einsum('bsf,fd->bsd', z1 * g, w_out)
    x = A_u
    g = _sigmoid((x * gate_w).sum(-1, keepdims=True))
    a = _A_ref(x, A_A_re, A_A_im, A_B_re, A_B_im, A_C_re, A_C_im, A_log_dt, A_Dskip)
    b = _B_ref(x, B_w_in, B_w_sp, B_b_sp, B_w_out)
    return g * a + (1 - g) * b
```

```cpp
#include <hip/hip_runtime.h>
#include <type_traits>
typedef unsigned int u32; typedef unsigned short u16; typedef unsigned long long u64;
using s16x4 = __attribute__((ext_vector_type(4))) short;
using bf16x8 = __attribute__((ext_vector_type(8))) __bf16;
using bf16x2 = __attribute__((ext_vector_type(2))) __bf16;
using f16x8 = __attribute__((ext_vector_type(8))) _Float16;
using h2 = __attribute__((ext_vector_type(2))) _Float16;
using f32x2 = __attribute__((ext_vector_type(2))) float;
using f32x4 = __attribute__((ext_vector_type(4))) float;
using f32x16 = __attribute__((ext_vector_type(16))) float;
using u32x4 = __attribute__((ext_vector_type(4))) u32;
using u32x2 = __attribute__((ext_vector_type(2))) u32;
#define LAS __attribute__((address_space(3)))
#define GAS __attribute__((address_space(1)))
#define DEV __device__ __forceinline__
#define RLX_AGENT __ATOMIC_RELAXED, __HIP_MEMORY_SCOPE_AGENT
#define SB() __builtin_amdgcn_sched_barrier(0)

constexpr u32 WS_EPOCH = 0;
constexpr u32 WS_FZ = 1024;
constexpr u32 WS_FH = 4096;
constexpr u32 WS_FA = 8192;
constexpr u32 WS_G = 16384;
constexpr u32 WS_STAT = 65536;
constexpr u32 WS_Z2N = 1u << 20;
constexpr u32 WS_H = 4u << 20;
constexpr u32 WS_A = 7u << 20;
constexpr u32 WS_END = 10u << 20;
constexpr u32 WS_DBG = 12u << 20;
constexpr int LDS_BYTES = 163840;
constexpr u32 SPIN_CAP = 1u << 18;

struct Params {
  const u16 *u, *Are, *Aim, *Bre, *Bim, *Cre, *Cim, *ldt, *dskip, *win, *wsp, *bsp, *wout, *gw;
  u16* out; char* ws;
};

template <int V> using IC = std::integral_constant<int, V>;
template <int B, int E, class F> DEV void sfor(F&& f) { if constexpr (B < E) { f(IC<B>{}); sfor<B + 1, E>(f); } }
DEV float asf(u32 x) { return __builtin_bit_cast(float, x); }
DEV u32 asu(float x) { return __builtin_bit_cast(u32, x); }
DEV f32x4 asf4(u32x4 x) { return __builtin_bit_cast(f32x4, x); }
DEV u32x4 asu4(f32x4 x) { return __builtin_bit_cast(u32x4, x); }
DEV float bflo(u32 w) { return asf(w << 16); }
DEV float bfhi(u32 w) { return asf(w & 0xffff0000u); }
DEV f32x2 bf2(u32 w) { return f32x2{bflo(w), bfhi(w)}; }
DEV u32 pk2(f32x2 v) { return __builtin_bit_cast(u32, __builtin_convertvector(v, bf16x2)); }
DEV u32 pk(float a, float b) { return pk2(f32x2{a, b}); }
DEV f32x16 mfma32(u32x4 a, u32x4 b, f32x16 c) { return __builtin_amdgcn_mfma_f32_32x32x16_bf16(__builtin_bit_cast(bf16x8, a), __builtin_bit_cast(bf16x8, b), c, 0, 0, 0); }
DEV f32x4 mfma16(u32x4 a, u32x4 b, f32x4 c) { return __builtin_amdgcn_mfma_f32_16x16x32_bf16(__builtin_bit_cast(bf16x8, a), __builtin_bit_cast(bf16x8, b), c, 0, 0, 0); }
DEV f32x4 mfma16h(u32x4 a, u32x4 b, f32x4 c) { return __builtin_amdgcn_mfma_f32_16x16x32_f16(__builtin_bit_cast(f16x8, a), __builtin_bit_cast(f16x8, b), c, 0, 0, 0); }
#define LP(T, a) ((LAS T*)(uintptr_t)(a))
DEV u32x4 lds128(u32 a) { return *LP(const u32x4, a); }
DEV u32x2 lds64(u32 a) { return *LP(const u32x2, a); }
DEV u32x2 ldstr(u32 a) { s16x4 r = __builtin_amdgcn_ds_read_tr16_b64_v4i16(LP(s16x4, a)); return __builtin_bit_cast(u32x2, r); }
DEV u32x4 ldstr2(u32 a0, u32 a1) { u32x2 x = ldstr(a0), y = ldstr(a1); return u32x4{x[0], x[1], y[0], y[1]}; }
DEV void sts128(u32 a, u32x4 v) { *LP(u32x4, a) = v; }
DEV void sts64(u32 a, u32x2 v) { *LP(u32x2, a) = v; }
DEV void sts32(u32 a, u32 v) { *LP(u32, a) = v; }
DEV void sts16(u32 a, u32 v) { *LP(u16, a) = (u16)v; }
template <int N> DEV void waitv() { asm volatile("s_waitcnt vmcnt(%0)" ::"n"(N > 63 ? 63 : N) : "memory"); }
DEV void bar() { asm volatile("s_waitcnt lgkmcnt(0)\n\ts_barrier" ::: "memory"); }
DEV void bar0() { asm volatile("s_barrier" ::: "memory"); }
DEV void glds1(const void* sbase, u32 v, u32 dst) {
  asm volatile("s_mov_b32 m0, %2\n\ts_nop 0\n\tglobal_load_lds_dwordx4 %0, %1" :: "v"(v), "s"(sbase), "s"(dst) : "memory");
}
template <int OFF> DEV void ldz(u32x4& d, u32 voff, __amdgpu_buffer_rsrc_t r, u32 soff) {
  asm volatile("buffer_load_dwordx4 %0, %1, %2, %3 offen offset:%4 sc1" : "=v"(d) : "v"(voff), "s"(r), "s"(soff), "n"(OFF) : "memory");
}
template <int N> DEV void waitz(u32x4 (&z)[4]) { asm volatile("s_waitcnt vmcnt(%4)" : "+v"(z[0]), "+v"(z[1]), "+v"(z[2]), "+v"(z[3]) : "n"(N > 63 ? 63 : N) : "memory"); }
template <int OFF> DEV void sts_tid(u32 v) { asm volatile("ds_write_addtid_b32 %0 offset:%1" :: "v"(v), "n"(OFF) : "memory"); }
DEV u64 ld_flag(const char* p) { return __hip_atomic_load((GAS u64*)p, RLX_AGENT); }
DEV void st_flag(char* p, u64 v) { __hip_atomic_store((GAS u64*)p, v, RLX_AGENT); }
DEV void poll(const char* p, u64 tag) {
#pragma nounroll
  for (u32 s = 0; s < SPIN_CAP; ++s) {
    if (__all(ld_flag(p) == tag)) break;
    __builtin_amdgcn_s_sleep(1);
  }
  asm volatile("" ::: "memory");
}
DEV f32x2 gelu2(f32x2 x) {
  constexpr float c0 = -2.0f * 0.7978845608028654f * 1.4426950408889634f, c1 = c0 * 0.044715f;
  f32x2 y = x * (x * x * c1 + c0);
  f32x2 e = f32x2{__builtin_amdgcn_exp2f(y[0]), __builtin_amdgcn_exp2f(y[1])} + 1.0f;
  return x * f32x2{__builtin_amdgcn_rcpf(e[0]), __builtin_amdgcn_rcpf(e[1])};
}
#define STAMP(i)
#define CSTAMP(i)

DEV h2 hmul(h2 a, h2 b) { h2 bs = {-b[1], b[0]}; return __builtin_elementwise_fma(a.yy, bs, a.xx * b); }
DEV u32 hbits(h2 a) { return __builtin_bit_cast(u32, a); }
struct cf { float r, i; };
DEV cf csq(cf a) { float t = a.i * a.i, u = a.r * a.i; asm("" : "+v"(t), "+v"(u)); return cf{__builtin_fmaf(a.r, a.r, -t), u + u}; }
DEV h2 toh(cf a) { return __builtin_convertvector(f32x2{a.r, a.i}, h2); }
DEV cf cexpv(float arl, float rev, float v) {
  float m = __builtin_amdgcn_exp2f(arl * v), f = __builtin_amdgcn_fractf(rev * v);
  return cf{m * __builtin_amdgcn_cosf(f), m * __builtin_amdgcn_sinf(f)};
}
DEV u32 wave_umax(u32 x) {
#define DPPMAX(ctrl, rm) x = max(x, (u32)__builtin_amdgcn_update_dpp(0, (int)x, ctrl, rm, 0xf, false))
  DPPMAX(0x111, 0xf); DPPMAX(0x112, 0xf); DPPMAX(0x114, 0xf); DPPMAX(0x118, 0xf); DPPMAX(0x142, 0xa); DPPMAX(0x143, 0xc);
  return __builtin_amdgcn_readlane(x, 63);
}
DEV int cidx(int l) {
  u32 bits = pk((float)l, 0.f) & 0xffffu;
  return l < 128 ? l + 16 : (int)bits - (134 << 7) + 128 + 16;
}

constexpr u32 S_SCR = 0, SCR_ST = 4608, SCR_ROW = 288, S_UR = 49152, UR_ST = 4608, S_KC = 95232, KC_CP = 1344, KC_ST = 5376;

DEV void kc_store(u32 kb, int e, float v) {
  u32 h = pk(v, 0.f);
#pragma unroll
  for (int k = 0; k < 4; ++k) sts16(kb + k * (KC_CP - 2) + 2 * e, h);
}
DEV void kc_write(u32 kb, const f32x4 (&acc)[3], float dsk, int lane) {
  int q = lane & 15, g = lane >> 4;
#pragma unroll
  for (int r = 0; r < 4; ++r) {
    int i = 4 * g + r;
    float v0 = acc[0][r]; if (r == 0 && lane == 0) v0 += dsk;
    kc_store(kb, 16 + 128 * (i >> 3) + (i & 7) + 8 * q, v0);
    int j = i - 8;
    kc_store(kb, 16 + (i < 8 ? 256 + 64 * (i >> 2) + 4 * q + (i & 3) : 384 + 32 * (j >> 1) + 2 * q + (j & 1)), acc[1][r]);
    kc_store(kb, 16 + (i < 8 ? 512 + 16 * i + q : (i == 8 && q == 0) ? 640 : 644), acc[2][r]);
  }
}
DEV u32 wadr(int e) { return (e & 3) * KC_CP + (e & ~3) * 2; }
DEV u32 wsel(int l, int e0) { return 0x01000100u + (cidx(l) - e0) * 0x00000202u + (cidx(l + 1) - e0) * 0x02020000u; }

DEV void ld_epoch(u64& e, const char* ws) { asm volatile("s_load_dwordx2 %0, %1, 0" : "=s"(e) : "s"(ws) : "memory"); }
DEV u64 mk_tag(u64& e) { asm volatile("" : "+s"(e)); u64 t = (e + 1) * 0x9E3779B97F4A7C15ull; return t ^ (t >> 29); }

DEV void ssm_role(const Params& p, int w, u64& epoch, u32& stv) {
  const int tid = threadIdx.x, lane = tid & 63, wv = __builtin_amdgcn_readfirstlane(tid >> 6);
  const int d0 = 10 * w;
  u32 prm[4][6]; u32 ld0, ld1, dk0, dk1;
  const int chx = 8 + (wv >= 3), cjx = wv >= 3 ? wv - 3 : wv;
  ld0 = p.ldt[d0 + wv]; ld1 = p.ldt[d0 + chx]; dk0 = p.dskip[d0 + wv]; dk1 = p.dskip[d0 + chx];
#pragma unroll
  for (int r = 0; r < 4; ++r) {
    int o = r < 3 ? (d0 + wv) * 192 + 64 * r + lane : (d0 + chx) * 192 + 64 * (cjx > 2 ? 0 : cjx) + lane;
    prm[r][0] = p.Are[o]; prm[r][1] = p.Aim[o]; prm[r][2] = p.Bre[o]; prm[r][3] = p.Bim[o]; prm[r][4] = p.Cre[o]; prm[r][5] = p.Cim[o];
  }
  asm volatile("" ::: "memory");
  u32x4 ua[8];
  const char* usrc = (const char*)p.u + (size_t)(tid >> 1) * 8 * 1280 + ((20 * w) & ~15) + (tid & 1) * 16;
  ua[0] = *(const u32x4*)usrc; ua[1] = *(const u32x4*)(usrc + 1280);
  ld_epoch(epoch, p.ws);
  if (tid < 320) sts128(S_UR + (tid >> 5) * UR_ST + 4096 + (tid & 31) * 16, u32x4{0, 0, 0, 0});
  if (tid < 80) sts128(S_KC + (tid >> 3) * KC_ST + ((tid >> 1) & 3) * KC_CP + (tid & 1) * 16, u32x4{0, 0, 0, 0});
  const int b = lane & 15, g = lane >> 4, lam = b - 15 + 8 * g;
  u32 a1[2][2], s1[2][4], a2[2][2], s2[2][4], a3, s3[4], a4[2], s4[2][4];
#pragma unroll
  for (int z = 0; z < 2; ++z) {
    const int L1 = z ? 256 : 288, C1 = z ? 256 : 272, L2 = z ? 512 : 576, C2 = z ? 384 : 400, L4 = z ? 1024 : 1088, C4 = z ? 512 : 520;
#pragma unroll
    for (int k = 0; k < 2; ++k) {
      int e = cidx(L1 + lam + 4 * k); a1[z][k] = wadr(e - C1); s1[z][2 * k] = wsel(L1 + lam + 4 * k, e); s1[z][2 * k + 1] = wsel(L1 + lam + 4 * k + 2, e);
      e = cidx(L2 + lam + 4 * k); a2[z][k] = wadr(e - C2); s2[z][2 * k] = wsel(L2 + lam + 4 * k, e); s2[z][2 * k + 1] = wsel(L2 + lam + 4 * k + 2, e);
    }
    int e = cidx(L4 + lam); a4[z] = wadr(e - C4);
#pragma unroll
    for (int k = 0; k < 4; ++k) s4[z][k] = wsel(L4 + lam + 2 * k, e);
  }
  a3 = a2[0][0]; s3[0] = s2[0][0]; s3[1] = s2[0][1]; s3[2] = wsel(580 + lam, cidx(576 + lam)); s3[3] = wsel(582 + lam, cidx(576 + lam));
  const u32 a0 = wadr(lam + 16), f0 = (240 - 16 * b + 8 * g) * 2;
  bar();
  const u32 scr = S_SCR + wv * SCR_ST, sr = scr + (lane & 15) * SCR_ROW + (lane >> 4) * 16;
  f32x4 kacc[3] = {};
  const int nr = wv < 6 ? 4 : 3;
  const float dt0 = bflo(pk(expf(bflo(ld0)), 0.f)), dt1 = bflo(pk(expf(bflo(ld1)), 0.f));
  asm volatile("s_mov_b32 m0, %0" :: "s"(scr) : "memory");
  STAMP(3);
#pragma nounroll
  for (int r = 0; r < nr; ++r) {
    if (r == 0) { ua[2] = *(const u32x4*)(usrc + 2560); ua[3] = *(const u32x4*)(usrc + 3840); ua[4] = *(const u32x4*)(usrc + 5120); }
    if (r == 1) { ua[5] = *(const u32x4*)(usrc + 6400); ua[6] = *(const u32x4*)(usrc + 7680); ua[7] = *(const u32x4*)(usrc + 8960); }
    const float dt = r < 3 ? dt0 : dt1;
    const float arl = dt * bflo(prm[0][0]) * 1.4426950408889634f, rev = dt * bflo(prm[0][1]) * 0.15915494309189535f;
    const float br = bflo(prm[0][2]), bi = bflo(prm[0][3]), cr = bflo(prm[0][4]), ci = bflo(prm[0][5]);
    const float kr = cr * br - ci * bi, ki = -(cr * bi + ci * br);
    const u32 ex = min(max(wave_umax(asu(fmaxf(fabsf(kr), fabsf(ki)))) & 0x7f800000u, 0x01000000u), 0x7e000000u);
    const float dn_ = asf(0x7f000000u - ex), up = asf(ex) * dt;
#pragma unroll
    for (int a = 0; a < 3; ++a)
#pragma unroll
      for (int c = 0; c < 6; ++c) prm[a][c] = prm[a + 1][c];
    h2 y[16], e[8], P[17], Ec[4];
    { cf t = cexpv(arl, -rev, 8.f); Ec[0] = toh(t); t = csq(t); Ec[1] = toh(t); t = csq(t); Ec[2] = toh(t); t = csq(t); Ec[3] = toh(t); }
    { cf t = cexpv(arl, rev, 1.f); e[1] = toh(t); t = csq(t); e[2] = toh(t); t = csq(t); e[4] = toh(t); }
    { cf t = cexpv(arl, rev, 128.f); P[1] = toh(t); t = csq(t); P[2] = toh(t); t = csq(t); P[4] = toh(t); t = csq(t); P[8] = toh(t); t = csq(t); P[16] = toh(t); }
    y[0] = toh(cf{kr * dn_, ki * dn_});
    u32x4 yf[4], x0[4], x1[4], x2[4];
    sfor<0, 4>([&](auto S) { sfor<0, (1 << S)>([&](auto Q) { y[(1 << S) + Q] = hmul(y[Q], Ec[S]); }); });
    e[3] = hmul(e[1], e[2]); P[3] = hmul(P[1], P[2]);
#pragma unroll
    for (int k = 1; k < 4; ++k) { e[4 + k] = hmul(e[k], e[4]); P[4 + k] = hmul(P[k], P[4]); }
    sfor<0, 16>([&](auto Q) { sts_tid<Q * SCR_ROW>(hbits(y[Q])); });
#pragma unroll
    for (int ks = 0; ks < 4; ++ks) yf[ks] = lds128(sr + ks * 64);
    sts_tid<0>(0x3c00u); sts_tid<8 * SCR_ROW>(hbits(P[1]));
    sfor<1, 8>([&](auto K) { sts_tid<K * SCR_ROW>(hbits(e[K])); sts_tid<(8 + K) * SCR_ROW>(hbits(hmul(e[K], P[1]))); });
#pragma unroll
    for (int ks = 0; ks < 4; ++ks) x0[ks] = lds128(sr + ks * 64);
    sfor<0, 8>([&](auto I) { constexpr int pp = 2 + I / 4, k = I & 3; sts_tid<I * SCR_ROW>(hbits(k ? hmul(P[pp], e[2 * k]) : P[pp])); });
    sfor<0, 4>([&](auto I) { sts_tid<(8 + 2 * I) * SCR_ROW>(hbits(P[4 + I])); sts_tid<(9 + 2 * I) * SCR_ROW>(hbits(hmul(P[4 + I], e[4]))); });
#pragma unroll
    for (int ks = 0; ks < 4; ++ks) x1[ks] = lds128(sr + ks * 64);
    sts_tid<0>(hbits(P[8])); sts_tid<8 * SCR_ROW>(hbits(P[16]));
    sfor<1, 8>([&](auto I) { sts_tid<I * SCR_ROW>(hbits(hmul(P[I], P[8]))); });
#pragma unroll
    for (int ks = 0; ks < 4; ++ks) x2[ks] = lds128(sr + ks * 64);
    asm volatile("s_waitcnt lgkmcnt(0)" ::: "memory");
    SB();
    f32x4 t[3] = {};
#pragma unroll
    for (int ks = 0; ks < 4; ++ks) { t[0] = mfma16h(x0[ks], yf[ks], t[0]); t[1] = mfma16h(x1[ks], yf[ks], t[1]); t[2] = mfma16h(x2[ks], yf[ks], t[2]); }
#pragma unroll
    for (int k = 0; k < 3; ++k) kacc[k] += t[k] * up;
    if (r == 2) {
      kc_write(S_KC + wv * KC_ST, kacc, bflo(dk0), lane); kacc[0] = kacc[1] = kacc[2] = f32x4{0, 0, 0, 0};
#pragma unroll
      for (int j = 0; j < 8; ++j) asm volatile("" : "+v"(ua[j]));
      const int c0 = (tid & 1) * 8 - (((20 * w) & 15) >> 1);
#pragma unroll
      for (int c = 0; c < 8; ++c) {
        const u32 sel = (c & 1) ? 0x03020706u : 0x01000504u;
        u32x4 v;
#pragma unroll
        for (int j = 0; j < 4; ++j) v[j] = __builtin_amdgcn_perm(ua[7 - 2 * j][c >> 1], ua[6 - 2 * j][c >> 1], sel);
        if ((u32)(c0 + c) < 10u) sts128(S_UR + (c0 + c) * UR_ST + (2040 - 8 * (tid >> 1)) * 2, v);
      }
    }
  }
  if (wv >= 6) {
    const int t2 = tid - 384, row = 32 * w + (t2 >> 2), part = t2 & 3;
    u32x4 gx[20], gv[20];
#pragma unroll
    for (int j = 0; j < 20; ++j) { gx[j] = *(const u32x4*)(p.u + row * 640 + (4 * j + part) * 8); gv[j] = *(const u32x4*)(p.gw + (4 * j + part) * 8); }
    SB();
    f32x2 s2_ = {0.f, 0.f};
#pragma unroll
    for (int j = 0; j < 20; ++j)
#pragma unroll
      for (int k = 0; k < 4; ++k) s2_ += bf2(gx[j][k]) * bf2(gv[j][k]);
    float sg = s2_[0] + s2_[1];
    sg += __shfl_xor(sg, 1); sg += __shfl_xor(sg, 2);
    if (part == 0) __hip_atomic_store((GAS float*)(p.ws + WS_G) + row, 1.0f / (1.0f + __expf(-sg)), RLX_AGENT);
  } else if (cjx != 0) {
#pragma unroll
    for (int k = 0; k < 3; ++k) sts128(scr + k * 1024 + lane * 16, asu4(kacc[k]));
  }
  STAMP(4);
  bar();
  if (wv < 6 && cjx == 0) {
#pragma unroll
    for (int k = 0; k < 3; ++k) kacc[k] += asf4(lds128(scr + SCR_ST + k * 1024 + lane * 16)) + asf4(lds128(scr + 2 * SCR_ST + k * 1024 + lane * 16));
    kc_write(S_KC + chx * KC_ST, kacc, bflo(dk1), lane);
  }
  bar();
  STAMP(1);
  const int jj = wv & 3, chb = 5 * (wv >> 2);
  f32x4 acc[8] = {};
  auto store_out = [&](int ch) {
#pragma unroll
    for (int R = 0; R < 8; ++R)
      __hip_atomic_store((GAS u64*)(p.ws + WS_A + (size_t)(d0 + ch) * 4096 + R * 512 + b * 32 + g * 8),
                         (u64)pk(acc[R][0], acc[R][1]) | ((u64)pk(acc[R][2], acc[R][3]) << 32), RLX_AGENT);
  };
  struct Adr { u32 f, w0, w1[2], w2[2], w4, x1[2], x2[2], x4; };
  auto mkadr = [&](int cu) {
    const int ch = chb + (cu >> 3), c = cu & 7;
    const u32 kb = S_KC + ch * KC_ST; const bool z = c == 0;
    Adr A;
    A.f = S_UR + ch * UR_ST + 64 * c + f0; A.w0 = kb + 64 * c + a0; A.w4 = kb + 1024 + 8 * c + a4[0]; A.x4 = z ? kb + 1024 + a4[1] : A.w4;
#pragma unroll
    for (int k = 0; k < 2; ++k) {
      A.w1[k] = kb + 512 + 32 * c + a1[0][k]; A.w2[k] = kb + 768 + 16 * c + a2[0][k];
      A.x1[k] = z ? kb + 512 + a1[1][k] : A.w1[k]; A.x2[k] = z ? kb + 768 + a2[1][k] : A.w2[k];
      asm volatile("" : "+v"(A.w1[k]), "+v"(A.w2[k]));
    }
    asm volatile("" : "+v"(A.f), "+v"(A.w0), "+v"(A.w4));
    return A;
  };
  auto rd = [&](auto ODD, auto T, const Adr& A, u32x2 (&W)[11], u32x4 (&F)[8]) {
    constexpr int t = T, o = ODD, j = t - 8;
    if constexpr (t < 8) F[t] = lds128(A.f + 64 * o + (7 - t) * 512);
    else if constexpr (j < 2) W[j] = lds64(A.w0 + 64 * o + 8 * j);
    else if constexpr (j < 4) W[j] = lds64(o ? A.w1[j - 2] + 32 : A.x1[j - 2]);
    else if constexpr (j < 6) W[j] = lds64(o ? A.w2[j - 4] + 16 : A.x2[j - 4]);
    else if constexpr (j == 6) W[j] = lds64(A.w2[0] + 128 + 16 * o);
    else if constexpr (j == 7) W[j] = lds64(o ? A.w4 + 8 : A.x4);
    else W[j] = lds64(A.w4 + 8 * o + 64 * (j - 7));
  };
  auto unit = [&](auto ODD, bool z, const Adr& An, const u32x2 (&W)[11], const u32x4 (&F)[8], u32x2 (&Wn)[11], u32x4 (&Fn)[8]) {
    u32x4 T[8];
    T[0] = u32x4{W[0][0], W[0][1], W[1][0], W[1][1]};
    SB();
    sfor<0, 36>([&](auto TT) {
      constexpr int t = TT, m = t < 8 ? 0 : t < 15 ? 1 : t < 21 ? 2 : t < 26 ? 3 : t < 30 ? 4 : t < 33 ? 5 : t < 35 ? 6 : 7, n = t - (8 * m - m * (m - 1) / 2);
      acc[m + n] = mfma16(T[m], F[n], acc[m + n]);
      if constexpr (t < 28) {
        constexpr int q = 1 + t / 4, k = t & 3, wi = q < 3 ? 2 * q + (k >> 1) : 3 + q;
        u32 sel;
        if constexpr (q == 1) sel = (!ODD && z) ? s1[1][k] : s1[0][k];
        else if constexpr (q == 2) sel = (!ODD && z) ? s2[1][k] : s2[0][k];
        else if constexpr (q == 3) sel = s3[k];
        else if constexpr (q == 4) sel = (!ODD && z) ? s4[1][k] : s4[0][k];
        else sel = s4[0][k];
        T[q][k] = __builtin_amdgcn_perm(W[wi][1], W[wi][0], sel);
      }
      if constexpr (t >= 12 && t < 31) rd(IC<1 - ODD>{}, IC<t - 12>{}, An, Wn, Fn);
      SB();
    });
  };
  u32x2 WA[11], WB[11]; u32x4 FA[8], FB[8];
  Adr A = mkadr(10 * jj);
  sfor<0, 19>([&](auto T) { rd(IC<0>{}, T, A, WA, FA); SB(); });
#pragma nounroll
  for (int i = 0; i < 10; i += 2) {
    const int cu = 10 * jj + i;
    unit(IC<0>{}, (cu & 7) == 0, A, WA, FA, WB, FB);
    A = mkadr(i < 8 ? cu + 2 : cu);
    unit(IC<1>{}, false, A, WB, FB, WA, FA);
    if ((cu & 7) == 6 && i < 8) {
      if (jj == 0) store_out(chb);
      else {
#pragma unroll
        for (int R = 0; R < 8; ++R) sts128(S_SCR + ((wv >> 2) * 3 + jj - 1) * 8192 + R * 1024 + lane * 16, asu4(acc[R]));
      }
#pragma unroll
      for (int R = 0; R < 8; ++R) acc[R] = f32x4{0, 0, 0, 0};
    }
  }
  STAMP(5);
  bar();
  if (jj < 3) {
#pragma unroll
    for (int R = 0; R < 8; ++R) acc[R] += asf4(lds128(S_SCR + ((wv >> 2) * 3 + jj) * 8192 + R * 1024 + lane * 16));
  }
  store_out(chb + jj + 1);
  bar();
  STAMP(2);
}

constexpr u32 G_SCR = 131072;
#define DZ 2
constexpr int need_of(int s) { return s < 10 ? 3 * (s / 2) + 1 + (s & 1) : s + 5; }
constexpr int cmin(int a, int b) { return a < b ? a : b; }
constexpr int p1_before(int s) { constexpr int t[21] = {16, 16, 18, 22, 24, 28, 30, 32, 34, 36, 46, 46, 48, 50, 52, 54, 56, 58, 60, 62, 66}; return t[s]; }
constexpr int p1_in(int s) { constexpr int t[20] = {0, 2, 4, 2, 4, 2, 2, 2, 2, 2, 0, 2, 2, 2, 2, 2, 2, 2, 2, 2}; return t[s]; }
constexpr int pcs_before(int i) { constexpr int t[17] = {0, 0, 2, 3, 5, 6, 8, 9, 11, 12, 14, 15, 16, 16, 16, 20, 24}; return t[i]; }
constexpr int zl_in(int s) { return s + DZ < 16 ? 4 : 0; }
constexpr int g2_wait(int i) {
  int nz = 0; for (int s = i - DZ; s < i; ++s) nz += (s < 0 ? 4 : zl_in(s) + pcs_before(s + 1) - pcs_before(s));
  if (i >= 8 && i < 16) {
    int sg = 0; while (pcs_before(sg + 1) < 2 * (i - 8) + 2) ++sg;
    int ns = 0; for (int s = sg + 1; s < i; ++s) ns += zl_in(s) + pcs_before(s + 1) - pcs_before(s);
    nz = cmin(nz, ns);
  }
  return nz;
}

__global__ __launch_bounds__(512, 2) void fused(Params p) {
  const int tid = threadIdx.x, lane = tid & 63, wv = __builtin_amdgcn_readfirstlane(tid >> 6);
  const int xcd = blockIdx.x & 7, idx = blockIdx.x >> 3, xm = xcd & 3, xn = xcd >> 2;
  u32 stv = 0;
  STAMP(0); CSTAMP(62);
  u64 epoch, tag;
  const __amdgpu_buffer_rsrc_t wsr = __builtin_amdgcn_make_buffer_rsrc(p.ws, 0, WS_END, 0x00020000);
  const int pm = xm * 8 + (idx & 7), dn = xn * 4 + (idx >> 3);
  const u32 dstw = wv * 2048;
  const int l31 = lane & 31, hh = lane >> 5, l15 = lane & 15, g4 = lane >> 4, q4 = l15 >> 2, p4 = lane & 3;
  char* myflag;
  u32 vo[2];
  auto mkvo = [&]() {
    int t = tid; asm volatile("" : "+v"(t));
    int u0 = (t >> 6) * 128 + (t & 63), u1 = u0 + 64; if (u1 >= 960) u1 -= 64;
    int r0 = u0 / 10, r1 = u1 / 10;
    vo[0] = (r0 ^ (((r0 >> 3) & 1) << 2)) * 1280 + (u0 - r0 * 10) * 16; vo[1] = (r1 ^ (((r1 >> 3) & 1) << 2)) * 1280 + (u1 - r1 * 10) * 16; };
  const char* obase = (const char*)p.wout + dn * 160;
  auto opiece = [&](auto Q) { constexpr int c = Q / 2, j = Q & 1; glds1(obase + c * 96 * 1280, vo[j], ((41 + c) & 7) * 16384 + j * 1024 + dstw); };
  if (idx >= 24) {
    const int w = xcd * 8 + (idx - 24);
    ssm_role(p, w, epoch, stv);
    tag = mk_tag(epoch);
    myflag = p.ws + WS_FA + w * 8;
    mkvo();
    sfor<0, 8>(opiece);
    waitv<8>();
    bar();
    if (tid == 0) st_flag(myflag, tag);
    sfor<8, 16>(opiece);
  } else {
    const int tn = xn * 3 + (idx >> 3);
    const int st = wv & 1, ft = wv >> 1;
    myflag = p.ws + WS_FH + (pm * 8 + tn) * 8;
    const u32 sl0 = 16 * (l15 ^ ((g4 << 2) | ((2 * wv) & 3))), sl1 = 16 * (l15 ^ ((g4 << 2) | ((2 * wv + 1) & 3)));
    const u32 row0 = 8 * wv + g4, row1 = row0 + 4;
    const u32 vx0 = row0 * 1280 + sl0, vx1 = row1 * 1280 + sl1, vw0 = row0 * 3072 + sl0, vw1 = row1 * 3072 + sl1, vs0 = row0 * 4096 + sl0, vs1 = row1 * 4096 + sl1;
    const char* xbase = (const char*)p.u + (size_t)pm * 64 * 1280;
    const char* wbase = (const char*)p.win + tn * 256;
    const char* sbase = (const char*)p.wsp + (size_t)pm * 64 * 4096;
    auto piece = [&](auto Q) {
      constexpr int c = Q / 2, j = Q & 1;
      const u32 dst = (c & 7) * 16384 + j * 1024 + dstw;
      if constexpr (c >= 41) opiece(IC<Q - 82>{});
      else if constexpr (c >= 25) glds1(sbase + (c - 25) * 256, j ? vs1 : vs0, dst);
      else if constexpr (c >= 15) glds1(wbase + (c - 15) * 64 * 3072, j ? vw1 : vw0, dst);
      else if constexpr (c % 3 == 1) glds1(xbase + (c / 3) * 256, j ? vx1 : vx0, dst);
      else glds1(wbase + 1536 + (2 * (c / 3) + (c % 3) / 2) * 64 * 3072, j ? vw1 : vw0, dst);
    };
    sfor<0, 8>([&](auto C) { piece(IC<2 * C>{}); piece(IC<2 * C + 1>{}); if constexpr (C >= 1 && C < 7) bar0(); });
    ld_epoch(epoch, p.ws);
    const u32 swr = ((l31 & 3) << 2) | ((l31 >> 2) & 3);
    const int blk = g4 & 1;
    u32 tro[2];
#pragma unroll
    for (int t = 0; t < 2; ++t) tro[t] = (8 * hh + 4 * t + q4) * 256 + 16 * ((4 * ft + 2 * blk + (p4 >> 1)) ^ ((q4 << 2) | ((2 * hh + t) & 3))) + 8 * (p4 & 1);
    u32 z2t[8], bspr;
    f32x16 zacc = {};
    {
      u32x4 xf[40], wf[2][4];
      const u32 xrow = (st * 32 + l31) * 256;
      auto mm = [&](auto S) { constexpr int s = S, j = s % 10;
#pragma unroll
        for (int ks = 0; ks < 4; ++ks) zacc = mfma32(wf[s & 1][ks], xf[4 * j + ks], zacc); };
      auto step = [&](auto S) {
        constexpr int s = S, j = s % 10, q0 = p1_before(s), nq = p1_in(s);
        waitv<p1_before(s) - 2 * need_of(s) - 2>();
        bar();
        STAMP(16 + s);
        constexpr u32 wsl = ((s < 10 ? 3 * (s / 2) + 2 * (s & 1) : s + 5) & 7) * 16384;
        sfor<0, 4>([&](auto KS) {
          constexpr int ks = KS;
          if constexpr (nq == 4) piece(IC<q0 + ks>{});
          if constexpr (nq == 2 && (ks & 1) == 0) piece(IC<q0 + ks / 2>{});
          if constexpr (ks < 2) {
#pragma unroll
            for (int k2 = 2 * ks; k2 < 2 * ks + 2; ++k2) {
              wf[s & 1][k2] = ldstr2(wsl + k2 * 4096 + tro[0], wsl + k2 * 4096 + tro[1]);
              if constexpr (s < 10) xf[4 * j + k2] = lds128(((3 * (s / 2) + 1) & 7) * 16384 + xrow + 16 * ((8 * (s & 1) + 2 * k2 + hh) ^ swr));
            }
          }
          SB();
          if constexpr (j > 0) zacc = mfma32(wf[(s - 1) & 1][ks], xf[4 * (j - 1) + ks], zacc);
          SB();
        });
      };
      sfor<0, 10>(step);
      mm(IC<9>{});
      STAMP(1);
      tag = mk_tag(epoch);
      { u32 zp[8]; f32x2 s1 = {0.f, 0.f}, s2 = {0.f, 0.f};
#pragma unroll
        for (int r = 0; r < 8; ++r) { f32x2 z = gelu2(f32x2{zacc[2 * r], zacc[2 * r + 1]}); zp[r] = pk2(z); s1 += z; s2 += z * z; }
        auto sw = __builtin_amdgcn_permlane32_swap(asu(s1[0] + s1[1]), asu(s2[0] + s2[1]), false, false);
        sts32(G_SCR + 16384 + (ft * 64 + st * 32 + l31) * 8 + hh * 4, asu(asf(sw[0]) + asf(sw[1])));
        const u32 tb = G_SCR + wv * 2048;
#pragma unroll
        for (int k = 0; k < 4; ++k) sts64(tb + l31 * 64 + 8 * ((2 * k + hh) ^ ((l31 >> 1) & 7)), u32x2{zp[2 * k], zp[2 * k + 1]});
        bar();
        if (wv == 0) {
          float a = 0.f, b = 0.f;
#pragma unroll
          for (int k = 0; k < 4; ++k) { u32x2 v = lds64(G_SCR + 16384 + (k * 64 + lane) * 8); a += asf(v[0]); b += asf(v[1]); }
          __builtin_amdgcn_raw_buffer_store_b128(u32x4{asu(a), (u32)tag, asu(b), (u32)tag}, wsr, WS_STAT + ((pm * 6 + tn) * 64 + lane) * 16, 0, 16);
        }
        STAMP(10);
        asm volatile("global_load_ushort %0, %1, %2" : "=v"(bspr) : "v"((pm * 64 + st * 32 + l31) * 2), "s"(p.bsp) : "memory");
#pragma unroll
        for (int k = 0; k < 4; ++k) { int row = 8 * (k & 2) + 8 * hh + 4 * (k & 1) + q4;
          u32x2 v = ldstr(tb + row * 64 + 8 * ((4 * blk + p4) ^ ((row >> 1) & 7))); z2t[2 * k] = v[0]; z2t[2 * k + 1] = v[1]; }
        sfor<19, 23>([&](auto C) { piece(IC<2 * C>{}); piece(IC<2 * C + 1>{}); if constexpr (C < 22) bar0(); });
        zacc = f32x16{};
      }
      sfor<10, 20>(step);
      mm(IC<19>{});
    }
    STAMP(2);
    if (wv == 0) {
      float a = 0.f, b = 0.f;
#pragma nounroll
      for (u32 s = 0; s < SPIN_CAP; ++s) {
        bool ok = true; a = 0.f; b = 0.f;
#pragma unroll
        for (int k = 0; k < 6; ++k) {
          u32x4 v = __builtin_amdgcn_raw_buffer_load_b128(wsr, WS_STAT + (pm * 6 * 64 + lane) * 16, k * 1024, 16);
          ok &= v[1] == (u32)tag && v[3] == (u32)tag; a += asf(v[0]); b += asf(v[2]);
        }
        if (__all(ok)) break;
        __builtin_amdgcn_s_sleep(1);
        asm volatile("" ::: "memory");
      }
      float m = a * (1.0f / 768.0f), var = fmaxf(b * (1.0f / 768.0f) - m * m, 0.f), rs = __builtin_amdgcn_rsqf(var + 1e-5f);
      sts64(G_SCR + 20480 + lane * 8, u32x2{asu(rs), asu(-m * rs)});
    }
    bar();
    STAMP(3);
#pragma unroll
    for (int ks = 0; ks < 2; ++ks) {
      u32x4 o;
#pragma unroll
      for (int j = 0; j < 4; ++j) {
        f32x4 c = asf4(lds128(G_SCR + 20480 + (st * 32 + 16 * ks + 8 * hh + 2 * j) * 8));
        o[j] = pk(bflo(z2t[4 * ks + j]) * c[0] + c[1], bfhi(z2t[4 * ks + j]) * c[2] + c[3]);
      }
      __builtin_amdgcn_raw_buffer_store_b128(o, wsr, WS_Z2N + (((4 * tn + ft) * 128 + 4 * pm + 2 * st + ks) * 64 + lane) * 16, 0, 16);
    }
    piece(IC<64>{}); piece(IC<65>{});
    SB();
    u32 z1p[8];
#pragma unroll
    for (int r = 0; r < 8; ++r) { z1p[r] = pk2(gelu2(f32x2{zacc[2 * r], zacc[2 * r + 1]})); asm volatile("" : "+v"(z1p[r])); }
    mkvo();
    waitv<2>();
    bar();
    if (wv == 0) {
      if (lane == 0) st_flag(p.ws + WS_FZ + (tn * 32 + pm) * 8, tag);
      STAMP(4);
      poll(p.ws + WS_FZ + (tn * 32 + l31) * 8, tag);
    }
    waitv<0>();
    asm volatile("" : "+v"(bspr));
    bar();
    STAMP(5);
    {
      const u32 zoff = WS_Z2N + (((4 * tn + ft) * 128 + 4 * st) * 64 + lane) * 16;
      u32x4 zf[DZ + 2][4], sf[2][8];
      sfor<0, DZ>([&](auto I) { sfor<0, 4>([&](auto K) { ldz<K * 1024>(zf[I][K], zoff, wsr, I * 8192); }); bar0(); });
      f32x16 acc[2] = {};
      u32 so[2][4];
#pragma unroll
      for (int k = 0; k < 4; ++k) { so[0][k] = l31 * 256 + 16 * ((8 * st + 2 * k + hh) ^ swr); so[1][k] = so[0][k] + 65536; asm volatile("" : "+v"(so[0][k]), "+v"(so[1][k])); }
      sfor<0, 17>([&](auto I) {
        constexpr int i = I, q0 = 66 + pcs_before(i < 16 ? i : 16), nq = i < 16 ? pcs_before(i + 1) - pcs_before(i) : 0;
        if constexpr (i >= 1) waitz<g2_wait(i)>(zf[(i - 1) % (DZ + 2)]);
        if constexpr (i >= 1 && i < 16) bar();
        STAMP(40 + i);
        constexpr u32 ss = ((25 + i) & 3) * 16384, sh = ((25 + i) >> 2) & 1;
        sfor<0, 8>([&](auto M) {
          constexpr int m = M;
          if constexpr (zl_in(i) > 0) {
            if constexpr (m == 0 || m == 1 || m == 3 || m == 5) { constexpr int k = m == 0 ? 0 : (m + 1) / 2; ldz<k * 1024>(zf[(i + DZ) % (DZ + 2)][k], zoff, wsr, (i + DZ) * 8192); }
            if constexpr (m == 2 && nq > 0) piece(IC<q0>{});
            if constexpr (m == 4 && nq > 1) piece(IC<q0 + 1>{});
            if constexpr (m == 6 && nq > 2) piece(IC<q0 + 2>{});
            if constexpr (m == 7 && nq > 3) piece(IC<q0 + 3>{});
          } else if constexpr ((m & 1) == 0 && m / 2 < nq) piece(IC<q0 + m / 2>{});
          if constexpr (i < 16 && m < 2) {
#pragma unroll
            for (int k = 2 * m; k < 2 * m + 2; ++k) { sf[i & 1][2 * k] = lds128(ss + so[sh][k]); sf[i & 1][2 * k + 1] = lds128(ss + 8192 + so[sh][k]); }
          }
          SB();
          if constexpr (i >= 1) acc[m & 1] = mfma32(zf[(i - 1) % (DZ + 2)][m / 2], sf[(i - 1) & 1][m], acc[m & 1]);
          SB();
        });
      });
      STAMP(6);
      { f32x16 snd = st ? acc[0] : acc[1];
#pragma unroll
        for (int k = 0; k < 4; ++k) sts128(G_SCR + wv * 4096 + k * 1024 + lane * 16, asu4(f32x4{snd[4 * k], snd[4 * k + 1], snd[4 * k + 2], snd[4 * k + 3]})); }
      bar();
      f32x16 G = st ? acc[1] : acc[0];
      const float bsp = bflo(bspr);
      u32 hp[8];
#pragma unroll
      for (int k = 0; k < 4; ++k) {
        f32x4 o = asf4(lds128(G_SCR + (wv ^ 1) * 4096 + k * 1024 + lane * 16));
        hp[2 * k] = pk2(bf2(z1p[2 * k]) * (f32x2{G[4 * k], G[4 * k + 1]} + f32x2{o[0], o[1]} + bsp));
        hp[2 * k + 1] = pk2(bf2(z1p[2 * k + 1]) * (f32x2{G[4 * k + 2], G[4 * k + 3]} + f32x2{o[2], o[3]} + bsp));
      }
#pragma unroll
      for (int k = 0; k < 2; ++k) {
        auto r0 = __builtin_amdgcn_permlane32_swap(hp[4 * k], hp[4 * k + 2], false, false);
        auto r1 = __builtin_amdgcn_permlane32_swap(hp[4 * k + 1], hp[4 * k + 3], false, false);
        __builtin_amdgcn_raw_buffer_store_b128(u32x4{r0[0], r1[0], r0[1], r1[1]}, wsr,
            WS_H + ((((4 * pm + 2 * st + (l31 >> 4)) * 24 + 4 * tn + ft) * 4 + 2 * k + hh) * 16 + l15) * 16, 0, 16);
      }
      STAMP(11);
      bar0();
      sfor<pcs_before(16) - 16, 16>(opiece);
      waitv<32 - pcs_before(16)>();
      bar();
      if (tid == 0) st_flag(myflag, tag);
    }
  }
  {
    STAMP(7);
    const int tt = wv >> 1, kh = wv & 1;
    if (wv == 0) poll(p.ws + WS_FA + ((lane & 8) ? 2 * pm + (lane & 1) : dn * 8 + (lane & 7)) * 8, tag);
    waitv<0>();
    bar();
    STAMP(12);
    const int trow = pm * 64 + tt * 16 + l15;
    u32 av[20]; float gt = 0.f;
    if (kh == 0) {
#pragma unroll
      for (int k = 0; k < 20; ++k) av[k] = __builtin_amdgcn_raw_buffer_load_b16(wsr, WS_A + (dn * 80 + 4 * g4) * 4096 + trow * 2, ((k >> 2) * 16 + (k & 3)) * 4096, 16);
      gt = asf(__builtin_amdgcn_raw_buffer_load_b32(wsr, WS_G + trow * 4, 0, 16));
    }
    const u32 ol0 = (8 * g4 + 4 * (g4 & 1) + q4) * 160 + p4 * 8, ol1 = (8 * g4 + 4 * ((g4 & 1) ^ 1) + q4) * 160 + p4 * 8;
    const u32 oa0 = ol0 + kh * 65536, oa1 = ol1 + kh * 65536, ob0 = ol0 + (kh ^ 1) * 65536, ob1 = ol1 + (kh ^ 1) * 65536;
    u32x4 wf[6][5];
    auto ldw = [&](auto K) { constexpr int k = K; constexpr u32 cb = ((1 + k / 3) & 3) * 16384 + (k % 3) * 32 * 160;
#pragma unroll
      for (int dtl = 0; dtl < 5; ++dtl) wf[k % 6][dtl] = ldstr2(cb + (k < 9 ? oa0 : ob0) + dtl * 32, cb + (k < 9 ? oa1 : ob1) + dtl * 32); };
    if (wv == 1) poll(p.ws + WS_FH + (pm * 8 + ((lane & 7) < 6 ? (lane & 7) : 0)) * 8, tag);
    bar0();
    STAMP(8);
    u32x4 hf[12];
#pragma unroll
    for (int k = 0; k < 12; ++k) hf[k] = __builtin_amdgcn_raw_buffer_load_b128(wsr, WS_H + (((4 * pm + tt) * 24 + 12 * kh) * 64 + lane) * 16 + (k & 3) * 1024, (k >> 2) * 4096, 16);
    SB();
    sfor<0, 6>(ldw);
    SB();
    f32x4 acc[5] = {};
    sfor<0, 12>([&](auto K) { constexpr int k = K;
#pragma unroll
      for (int dtl = 0; dtl < 5; ++dtl) acc[dtl] = mfma16(wf[k % 6][dtl], hf[k], acc[dtl]);
      SB();
      if constexpr (k < 6) ldw(IC<k + 6>{});
      SB();
    });
    STAMP(13);
    if (kh == 1) {
#pragma unroll
      for (int k = 0; k < 5; ++k) sts128(G_SCR + tt * 5120 + k * 1024 + lane * 16, asu4(acc[k]));
    }
    bar();
    if (kh == 0) {
#pragma unroll
      for (int k = 0; k < 5; ++k) {
        f32x4 o = acc[k] + asf4(lds128(G_SCR + tt * 5120 + k * 1024 + lane * 16));
        float r[4];
#pragma unroll
        for (int j = 0; j < 4; ++j) r[j] = o[j] + gt * (bflo(av[4 * k + j]) - o[j]);
        *(u32x2*)(p.out + trow * 640 + dn * 80 + k * 16 + 4 * g4) = u32x2{pk(r[0], r[1]), pk(r[2], r[3])};
      }
    }
    STAMP(9); CSTAMP(63);
    if (blockIdx.x == 0 && wv == 1) {
      poll(p.ws + WS_FA + lane * 8, tag);
      if (lane == 0) st_flag(p.ws + WS_EPOCH, epoch + 1);
    }
  }
}

extern "C" void kernel_launch(void* const* d_in, const int* in_sizes, int n_in, void* d_out, int out_size, void* d_ws, size_t ws_size, hipStream_t stream) {
  Params p;
  p.u = (const u16*)d_in[0]; p.Are = (const u16*)d_in[1]; p.Aim = (const u16*)d_in[2]; p.Bre = (const u16*)d_in[3]; p.Bim = (const u16*)d_in[4];
  p.Cre = (const u16*)d_in[5]; p.Cim = (const u16*)d_in[6]; p.ldt = (const u16*)d_in[7]; p.dskip = (const u16*)d_in[8];
  p.win = (const u16*)d_in[10]; p.wsp = (const u16*)d_in[11]; p.bsp = (const u16*)d_in[12]; p.wout = (const u16*)d_in[13]; p.gw = (const u16*)d_in[14];
  p.out = (u16*)d_out; p.ws = (char*)d_ws;
  (void)hipFuncSetAttribute((const void*)fused, hipFuncAttributeMaxDynamicSharedMemorySize, LDS_BYTES);
  fused<<<256, 512, LDS_BYTES, stream>>>(p);
}
```

```cpp
#include <hip/hip_runtime.h>
#include <type_traits>
typedef unsigned int u32; typedef unsigned short u16; typedef unsigned long long u64;
using s16x4 = __attribute__((ext_vector_type(4))) short;
using bf16x8 = __attribute__((ext_vector_type(8))) __bf16;
using bf16x2 = __attribute__((ext_vector_type(2))) __bf16;
using f32x2 = __attribute__((ext_vector_type(2))) float;
using f32x4 = __attribute__((ext_vector_type(4))) float;
using f32x16 = __attribute__((ext_vector_type(16))) float;
using u32x4 = __attribute__((ext_vector_type(4))) u32;
using u32x2 = __attribute__((ext_vector_type(2))) u32;
#define LAS __attribute__((address_space(3)))
#define GAS __attribute__((address_space(1)))
#define DEV __device__ __forceinline__
#define RLX_AGENT __ATOMIC_RELAXED, __HIP_MEMORY_SCOPE_AGENT
#define SB() __builtin_amdgcn_sched_barrier(0)

constexpr u32 WS_EPOCH = 0;
constexpr u32 WS_FZ = 524288;
constexpr u32 WS_FU = 786432;
constexpr u32 WS_FW = 6144;
constexpr u32 WS_FA = 8192;
constexpr u32 WS_G = 16384;
constexpr u32 WS_STAT = 65536;
constexpr u32 WS_Z2N = 2u << 20;
constexpr u32 WS_H = 5u << 20;
constexpr u32 WS_A = 8u << 20;
constexpr u32 WS_UT = 11u << 20;
constexpr u32 WS_WP = 14u << 20;
constexpr u32 WS_END = 15u << 20;
constexpr int LDS_BYTES = 163840;
constexpr u32 SPIN_CAP = 1u << 18;

struct Params {
  const u16 *u, *win, *wsp, *wout, *bsp; char* ws; u16* out;
  const u16 *gw, *Are, *Aim, *Bre, *Bim, *Cre, *Cim, *ldt, *dskip;
};

template <int V> using IC = std::integral_constant<int, V>;
template <int B, int E, class F> DEV void sfor(F&& f) { if constexpr (B < E) { f(IC<B>{}); sfor<B + 1, E>(f); } }
DEV float asf(u32 x) { return __builtin_bit_cast(float, x); }
DEV u32 asu(float x) { return __builtin_bit_cast(u32, x); }
DEV f32x4 asf4(u32x4 x) { return __builtin_bit_cast(f32x4, x); }
DEV u32x4 asu4(f32x4 x) { return __builtin_bit_cast(u32x4, x); }
DEV float bflo(u32 w) { return asf(w << 16); }
DEV float bfhi(u32 w) { return asf(w & 0xffff0000u); }
DEV f32x2 bf2(u32 w) { return f32x2{bflo(w), bfhi(w)}; }
DEV u32 pk2(f32x2 v) { return __builtin_bit_cast(u32, __builtin_convertvector(v, bf16x2)); }
DEV u32 pk(float a, float b) { return pk2(f32x2{a, b}); }
DEV f32x16 mfma32(u32x4 a, u32x4 b, f32x16 c) { return __builtin_amdgcn_mfma_f32_32x32x16_bf16(__builtin_bit_cast(bf16x8, a), __builtin_bit_cast(bf16x8, b), c, 0, 0, 0); }
DEV void mfma32v(f32x16& c, u32x4 a, u32x4 b) { asm volatile("v_mfma_f32_32x32x16_bf16 %0, %1, %2, %0" : "+v"(c) : "v"(a), "v"(b)); }
DEV void mfma32z(f32x16& c, u32x4 a, u32x4 b) { asm volatile("v_mfma_f32_32x32x16_bf16 %0, %1, %2, 0" : "=&v"(c) : "v"(a), "v"(b)); }
DEV void mfma_done(f32x16& c) { asm volatile("s_nop 13" : "+v"(c)); }
DEV f32x4 mfma16(u32x4 a, u32x4 b, f32x4 c) { return __builtin_amdgcn_mfma_f32_16x16x32_bf16(__builtin_bit_cast(bf16x8, a), __builtin_bit_cast(bf16x8, b), c, 0, 0, 0); }
#define LP(T, a) ((LAS T*)(uintptr_t)(a))
DEV u32x4 lds128(u32 a) { return *LP(const u32x4, a); }
DEV u32x2 lds64(u32 a) { return *LP(const u32x2, a); }
DEV u32x2 ldstr(u32 a) { s16x4 r = __builtin_amdgcn_ds_read_tr16_b64_v4i16(LP(s16x4, a)); return __builtin_bit_cast(u32x2, r); }
DEV u32x4 ldstr2(u32 a0, u32 a1) { u32x2 x = ldstr(a0), y = ldstr(a1); return u32x4{x[0], x[1], y[0], y[1]}; }
DEV void sts128(u32 a, u32x4 v) { *LP(u32x4, a) = v; }
DEV void sts64(u32 a, u32x2 v) { *LP(u32x2, a) = v; }
DEV void sts32(u32 a, u32 v) { *LP(u32, a) = v; }
DEV void sts16(u32 a, u32 v) { *LP(u16, a) = (u16)v; }
template <int N> DEV void waitv() { asm volatile("s_waitcnt vmcnt(%0)" ::"n"(N > 63 ? 63 : N) : "memory"); }
DEV void bar() { asm volatile("s_waitcnt lgkmcnt(0)\n\ts_barrier" ::: "memory"); }
DEV int lane_now() { int l; asm volatile("v_mbcnt_lo_u32_b32 %0, -1, 0\n\tv_mbcnt_hi_u32_b32 %0, -1, %0" : "=v"(l)); return l; }
DEV void bar0() { asm volatile("s_barrier" ::: "memory"); }
DEV void glds1(const void* sbase, u32 v, u32 dst) {
  asm volatile("s_mov_b32 m0, %2\n\ts_nop 0\n\tglobal_load_lds_dwordx4 %0, %1" :: "v"(v), "s"(sbase), "s"(dst) : "memory");
}
DEV void glds1s(const void* sbase, u32 v, u32 dst) {
  asm volatile("s_mov_b32 m0, %2\n\ts_nop 0\n\tglobal_load_lds_dwordx4 %0, %1 sc1" :: "v"(v), "s"(sbase), "s"(dst) : "memory");
}
constexpr u32 POISON = 0xffffffffu;
DEV u32 unp(u32 x) { return min(x, POISON - 1); }
DEV u32 max4(u32x4 v, u32 m) { return max(max(max(v[0], v[1]), m), max(v[2], v[3])); }
DEV bool stale(const u32x4 (&z)[4]) { return __any(max4(z[3], max4(z[2], max4(z[1], max4(z[0], 0)))) == POISON); }
DEV void st16(u32x4 v, __amdgpu_buffer_rsrc_t r, u32 voff, u32 soff) {
  asm volatile("buffer_store_dwordx4 %0, %1, %2, %3 offen sc1\n\ts_nop 1" :: "v"(v), "v"(voff), "s"(r), "s"(soff) : "memory");
}
template <int OFF> DEV void ldz(u32x4& d, u32 voff, __amdgpu_buffer_rsrc_t r, u32 soff) {
  asm volatile("buffer_load_dwordx4 %0, %1, %2, %3 offen offset:%4 sc1" : "=v"(d) : "v"(voff), "s"(r), "s"(soff), "n"(OFF) : "memory");
}
template <int OFF> DEV void ldzr(u32x4& d, u32 voff, __amdgpu_buffer_rsrc_t r, u32 soff) {
  asm volatile("buffer_load_dwordx4 %0, %1, %2, %3 offen offset:%4 sc1" : "+v"(d) : "v"(voff), "s"(r), "s"(soff), "n"(OFF) : "memory");
}
template <int N> DEV void waitz(u32x4 (&z)[4]) { asm volatile("s_waitcnt vmcnt(%4)" : "+v"(z[0]), "+v"(z[1]), "+v"(z[2]), "+v"(z[3]) : "n"(N > 63 ? 63 : N) : "memory"); }
DEV u64 ld_flag(const char* p) { return __hip_atomic_load((GAS u64*)p, RLX_AGENT); }
DEV bool flag_ok(__amdgpu_buffer_rsrc_t r, u32 off, u64 tag) { const u32x2 v = __builtin_amdgcn_raw_buffer_load_b64(r, off, 0, 16); return v[0] == (u32)tag && v[1] == (u32)(tag >> 32); }
DEV void st_flag(char* p, u64 v) { __hip_atomic_store((GAS u64*)p, v, RLX_AGENT); }
DEV void poll(const char* p, u64 tag) {
#pragma nounroll
  for (u32 s = 0; s < SPIN_CAP; ++s) {
    if (__all(ld_flag(p) == tag)) break;
    __builtin_amdgcn_s_sleep(1);
  }
  asm volatile("" ::: "memory");
}
DEV f32x2 gelu2(f32x2 x) {
  constexpr float c0 = -2.0f * 0.7978845608028654f * 1.4426950408889634f, c1 = c0 * 0.044715f;
  f32x2 y = x * (x * x * c1 + c0);
  f32x2 e = f32x2{__builtin_amdgcn_exp2f(y[0]), __builtin_amdgcn_exp2f(y[1])} + 1.0f;
  return x * f32x2{__builtin_amdgcn_rcpf(e[0]), __builtin_amdgcn_rcpf(e[1])};
}

constexpr u32 G_SCR = 0, RB = 32768;
constexpr u32 SCR_ST = 4608, SCR_ROW = 288, S_CT = 27648, S_PRM = 36864, S_WO0 = 61440, S_WO1 = 71680;
constexpr u32 S_UR = 81920, UR_ST = 4608, S_KC = 128000, KC_CP = 832, KC_T2 = 1664, KC_T3 = 2176, KC_DUM = 2560, KC_ST = 2688, S_GW = 154880;

struct KcAdr { u32 t0, c1a, c1b, s1x, s1a, s1b, t2x, t2; };
DEV KcAdr kc_adr(int lane) {
  const int q = lane & 15, g = lane >> 4;
  KcAdr A;
  A.t0 = 2 * (16 + 128 * (g >> 1) + 4 * (g & 1) + 8 * q);
  const u32 n1 = 2 * (16 + 256 + 64 * g + 4 * q);
  A.c1a = lane <= 32 ? n1 : KC_DUM; A.c1b = g < 2 ? n1 + 4 : KC_DUM;
  A.s1a = g < 2 ? KC_CP - 2 + n1 : KC_T2 + 2 * (59 + 64 * (g - 2) + 2 * q); A.s1b = A.s1a + (g < 2 ? 4 : 64); A.s1x = lane == 32 ? KC_CP - 2 + n1 : A.s1a;
  A.t2 = (q < 8 || lane == 8) ? KC_T3 + 2 * (29 + 16 * q + 4 * g) : KC_DUM + 2; A.t2x = lane == 0 ? KC_T2 + 2 * 187 : A.t2;
  return A;
}
DEV void kc_zero(u32 kb, int lane) { if (lane < 57) sts128(kb + (lane < 53 ? KC_T2 + lane * 16 : ((lane - 53) >> 1) * KC_CP + ((lane - 53) & 1) * 16), u32x4{0, 0, 0, 0}); }
struct KcPk { u32 v[6]; };
DEV KcPk kc_pack(const f32x16& a, float dsk, int lane) {
  return KcPk{{pk(lane == 0 ? a[0] + dsk : a[0], a[1]), pk(a[2], a[3]), pk(a[4], a[5]), pk(a[6], a[7]), pk(a[8], a[9]), pk(a[10], a[11])}};
}
DEV void kc_write(u32 kb, const KcAdr& A, const KcPk& P) {
  const u32 p0 = P.v[0], p1 = P.v[1], p2 = P.v[2], p3 = P.v[3], p4 = P.v[4], p5 = P.v[5];
  sts64(kb + A.t0, u32x2{p0, p1}); sts32(kb + A.c1a, p2); sts32(kb + A.c1b, p3);
  const u32 k1 = kb + KC_CP - 2;
  sts16(k1 + A.t0, p0); sts32(k1 + A.t0 + 2, __builtin_amdgcn_alignbit(p1, p0, 16)); sts16(k1 + A.t0 + 6, p1 >> 16);
  sts16(kb + A.s1x, p2); sts16(kb + A.s1a + 2, p2 >> 16); sts16(kb + A.s1b, p3); sts16(kb + A.s1b + 2, p3 >> 16);
  sts16(kb + A.t2x, p4); sts32(kb + A.t2 + 2, __builtin_amdgcn_alignbit(p5, p4, 16)); sts16(kb + A.t2 + 6, p5 >> 16);
}
DEV void touch(const void* base, u32 off, u32 dummy) {
  asm volatile("s_mov_b32 m0, %2\n\ts_nop 0\n\tglobal_load_lds_dword %0, %1" :: "v"(off), "s"(base), "s"(dummy) : "memory");
}

DEV void ld_epoch(u32x2& e, const char* ws) { asm volatile("global_load_dwordx2 %0, %1, %2 sc1" : "=v"(e) : "v"(0), "s"(ws) : "memory"); }
DEV u64 get_epoch(u32x2& e) { asm volatile("" : "+v"(e)); return ((u64)(u32)__builtin_amdgcn_readfirstlane(e[1]) << 32) | (u32)__builtin_amdgcn_readfirstlane(e[0]); }
DEV u64 rd_epoch(const u32x2& e) {
  u32 lo, hi; asm volatile("v_readfirstlane_b32 %0, %2\n\tv_readfirstlane_b32 %1, %3" : "=&s"(lo), "=&s"(hi) : "v"(e[0]), "v"(e[1])); return ((u64)hi << 32) | lo; }
DEV u64 mk_tag(u64 e) { u64 t = (e + 1) * 0x9E3779B97F4A7C15ull; return t ^ (t << 13) ^ 0x5851F42D4C957F2Dull; }

constexpr int tap_entry(int l) {
  if (l < 256) return l + 16;
  int sh = l < 512 ? 1 : l < 1024 ? 2 : 3, q = l >> sh, rem = l & ((1 << sh) - 1), half = 1 << (sh - 1);
  if (rem > half || (rem == half && (q & 1))) ++q;
  return 128 * sh + q + 16;
}
struct ConvTab { u32 v[13][64][4]; };
constexpr ConvTab make_tab() {
  ConvTab T = {};
  for (int lane = 0; lane < 64; ++lane) {
    const int b = lane & 15, g = lane >> 4, lam = b - 15 + 8 * g;
    u32 c[52] = {};
    const int off[6] = {24, 16, 24, 16, 16, 16}, ctr[6] = {-16, -8, -16, -4, -8, -12}, hw[6] = {4, 3, 2, 1, 2, 1};
    for (int m = 0; m < 6; ++m)
      for (int i = 0; i < 8; ++i) { const int r = -off[m] + 31 - (8 * g + i) - (b + ctr[m]); if (r <= hw[m] && r >= -hw[m]) c[4 * m + i / 2] |= 0x3f80u << (16 * (i & 1)); }
    auto half = [&](int l, int e0) { return l > 514 ? 0x0c0cu : (u32)(0x0100 + (tap_entry(l) - e0) * 0x0202); };
    auto sel = [&](int l, int e0) { return half(l, e0) | (half(l + 1, e0) << 16); };
    auto adr = [&](int e) { return (u32)((e & 1) * (int)KC_CP + (e & ~1) * 2); };
    for (int k = 0; k < 2; ++k) {
      int e = tap_entry(288 + lam + 4 * k); c[42 + k] = adr(e - 16); c[24 + 2 * k] = sel(288 + lam + 4 * k, e); c[25 + 2 * k] = sel(290 + lam + 4 * k, e);
      e = tap_entry(256 + lam + 4 * k); c[44 + k] = adr(e); c[28 + 2 * k] = sel(256 + lam + 4 * k, e); c[29 + 2 * k] = sel(258 + lam + 4 * k, e);
      const int l2 = 512 + lam + 4 * k; e = tap_entry(l2 > 514 ? 514 : l2) & ~1; c[46 + k] = adr(e); c[32 + 2 * k] = sel(l2, e); c[33 + 2 * k] = sel(l2 + 2, e);
    }
    for (int i = 0; i < 8; ++i) { const int l = 1024 + lam + i; c[36 + i / 2] |= ((l == 1027 || l == 1028) ? 0x0302u : 0x0c0cu) << (16 * (i & 1)); }
    c[40] = (240 - 16 * b + 8 * g) * 2; c[41] = adr(lam + 16);
    c[48] = KC_T2 + 8 * b - 32 * g + 96; c[49] = KC_T3 + 4 * b - 16 * g + 48; c[50] = KC_T2 + 372;
    for (int i = 0; i < 52; ++i) T.v[i / 4][lane][i & 3] = c[i];
  }
  return T;
}
static __device__ const __attribute__((aligned(16))) ConvTab conv_tab = make_tab();
using u32x16 = __attribute__((ext_vector_type(16))) u32;
using u32x32 = __attribute__((ext_vector_type(32))) u32;
using f32x32 = __attribute__((ext_vector_type(32))) float;
#define CONV_ASM \
  "s_cmp_lg_u32 %[p], 0\n\t" \
  "s_cbranch_scc1 Lce%=\n\t" \
  "ds_read2_b32 v[176:177], v97 offset0:0 offset1:1\n\t" \
  "ds_read2_b32 v[178:179], v97 offset0:2 offset1:3\n\t" \
  "ds_read_b128 v[112:115], v96 offset:3584\n\t" \
  "ds_read2_b32 v[180:181], v100 offset0:0 offset1:1\n\t" \
  "ds_read2_b32 v[182:183], v101 offset0:0 offset1:1\n\t" \
  "ds_read_b128 v[116:119], v96 offset:3072\n\t" \
  "ds_read_b128 v[120:123], v96 offset:2560\n\t" \
  "ds_read_b128 v[124:127], v96 offset:2048\n\t" \
  "ds_read_b128 v[128:131], v96 offset:1536\n\t" \
  "ds_read_b128 v[132:135], v96 offset:1024\n\t" \
  "ds_read_b128 v[136:139], v96 offset:512\n\t" \
  "ds_read_b128 v[140:143], v96 offset:0\n\t" \
  "ds_read2_b32 v[200:201], v102 offset0:0 offset1:1\n\t" \
  "ds_read2_b32 v[202:203], v103 offset0:0 offset1:1\n\t" \
  "ds_read2_b32 v[204:205], v106 offset0:0 offset1:1\n\t" \
  "Lcp0%=:\n\t" \
  "s_waitcnt lgkmcnt(11)\n\t" \
  "v_mfma_f32_16x16x32_bf16 v[16:19], v[176:179], v[112:115], v[16:19]\n\t" \
  "s_waitcnt lgkmcnt(9)\n\t" \
  "v_mfma_f32_16x16x32_bf16 v[20:23], v[176:179], v[116:119], v[20:23]\n\t" \
  "v_perm_b32 v192, v181, v180, v76\n\t" \
  "s_waitcnt lgkmcnt(8)\n\t" \
  "v_mfma_f32_16x16x32_bf16 v[24:27], v[176:179], v[120:123], v[24:27]\n\t" \
  "v_perm_b32 v193, v181, v180, v77\n\t" \
  "s_waitcnt lgkmcnt(7)\n\t" \
  "v_mfma_f32_16x16x32_bf16 v[28:31], v[176:179], v[124:127], v[28:31]\n\t" \
  "v_perm_b32 v194, v183, v182, v78\n\t" \
  "s_waitcnt lgkmcnt(6)\n\t" \
  "v_mfma_f32_16x16x32_bf16 v[32:35], v[176:179], v[128:131], v[32:35]\n\t" \
  "v_perm_b32 v195, v183, v182, v79\n\t" \
  "s_waitcnt lgkmcnt(5)\n\t" \
  "v_mfma_f32_16x16x32_bf16 v[36:39], v[176:179], v[132:135], v[36:39]\n\t" \
  "s_waitcnt lgkmcnt(2)\n\t" \
  "v_perm_b32 v208, v201, v200, v80\n\t" \
  "v_mfma_f32_16x16x32_bf16 v[40:43], v[176:179], v[136:139], v[40:43]\n\t" \
  "v_perm_b32 v209, v201, v200, v81\n\t" \
  "v_mfma_f32_16x16x32_bf16 v[44:47], v[176:179], v[140:143], v[44:47]\n\t" \
  "s_waitcnt lgkmcnt(1)\n\t" \
  "v_perm_b32 v210, v203, v202, v82\n\t" \
  "v_mfma_f32_16x16x32_bf16 v[20:23], v[192:195], v[112:115], v[20:23]\n\t" \
  "v_perm_b32 v211, v203, v202, v83\n\t" \
  "ds_read2_b32 v[184:185], v97 offset0:16 offset1:17\n\t" \
  "v_mfma_f32_16x16x32_bf16 v[24:27], v[192:195], v[116:119], v[24:27]\n\t" \
  "s_waitcnt lgkmcnt(1)\n\t" \
  "v_perm_b32 v212, v205, v204, v84\n\t" \
  "ds_read2_b32 v[186:187], v97 offset0:18 offset1:19\n\t" \
  "v_mfma_f32_16x16x32_bf16 v[28:31], v[192:195], v[120:123], v[28:31]\n\t" \
  "v_perm_b32 v213, v205, v204, v85\n\t" \
  "ds_read_b128 v[144:147], v96 offset:3648\n\t" \
  "v_mfma_f32_16x16x32_bf16 v[32:35], v[192:195], v[124:127], v[32:35]\n\t" \
  "v_perm_b32 v214, v205, v204, v86\n\t" \
  "ds_read2_b32 v[188:189], v98 offset0:8 offset1:9\n\t" \
  "v_mfma_f32_16x16x32_bf16 v[36:39], v[192:195], v[128:131], v[36:39]\n\t" \
  "v_perm_b32 v215, v205, v204, v87\n\t" \
  "ds_read2_b32 v[190:191], v99 offset0:8 offset1:9\n\t" \
  "v_mfma_f32_16x16x32_bf16 v[40:43], v[192:195], v[132:135], v[40:43]\n\t" \
  "ds_read_b128 v[148:151], v96 offset:3136\n\t" \
  "v_mfma_f32_16x16x32_bf16 v[44:47], v[192:195], v[136:139], v[44:47]\n\t" \
  "ds_read_b128 v[152:155], v96 offset:2624\n\t" \
  "v_mfma_f32_16x16x32_bf16 v[24:27], v[208:211], v[112:115], v[24:27]\n\t" \
  "ds_read_b128 v[156:159], v96 offset:2112\n\t" \
  "v_mfma_f32_16x16x32_bf16 v[28:31], v[208:211], v[116:119], v[28:31]\n\t" \
  "ds_read_b128 v[160:163], v96 offset:1600\n\t" \
  "v_mfma_f32_16x16x32_bf16 v[32:35], v[208:211], v[120:123], v[32:35]\n\t" \
  "ds_read_b128 v[164:167], v96 offset:1088\n\t" \
  "v_mfma_f32_16x16x32_bf16 v[36:39], v[208:211], v[124:127], v[36:39]\n\t" \
  "ds_read_b128 v[168:171], v96 offset:576\n\t" \
  "v_mfma_f32_16x16x32_bf16 v[40:43], v[208:211], v[128:131], v[40:43]\n\t" \
  "ds_read_b128 v[172:175], v96 offset:64\n\t" \
  "v_mfma_f32_16x16x32_bf16 v[44:47], v[208:211], v[132:135], v[44:47]\n\t" \
  "v_mfma_f32_16x16x32_bf16 v[32:35], v[212:215], v[112:115], v[32:35]\n\t" \
  "v_mfma_f32_16x16x32_bf16 v[36:39], v[212:215], v[116:119], v[36:39]\n\t" \
  "v_mfma_f32_16x16x32_bf16 v[40:43], v[212:215], v[120:123], v[40:43]\n\t" \
  "v_mfma_f32_16x16x32_bf16 v[44:47], v[212:215], v[124:127], v[44:47]\n\t" \
  "s_waitcnt lgkmcnt(2)\n\t" \
  "v_mfma_f32_16x16x32_bf16 v[16:19], v[184:187], v[144:147], v[16:19]\n\t" \
  "ds_read2_b32 v[176:177], v97 offset0:32 offset1:33\n\t" \
  "v_mfma_f32_16x16x32_bf16 v[20:23], v[184:187], v[148:151], v[20:23]\n\t" \
  "v_perm_b32 v196, v189, v188, v72\n\t" \
  "ds_read2_b32 v[178:179], v97 offset0:34 offset1:35\n\t" \
  "v_mfma_f32_16x16x32_bf16 v[24:27], v[184:187], v[152:155], v[24:27]\n\t" \
  "v_perm_b32 v197, v189, v188, v73\n\t" \
  "ds_read_b128 v[112:115], v96 offset:3712\n\t" \
  "v_mfma_f32_16x16x32_bf16 v[28:31], v[184:187], v[156:159], v[28:31]\n\t" \
  "v_perm_b32 v198, v191, v190, v74\n\t" \
  "ds_read2_b32 v[180:181], v98 offset0:16 offset1:17\n\t" \
  "v_mfma_f32_16x16x32_bf16 v[32:35], v[184:187], v[160:163], v[32:35]\n\t" \
  "v_perm_b32 v199, v191, v190, v75\n\t" \
  "ds_read2_b32 v[182:183], v99 offset0:16 offset1:17\n\t" \
  "v_mfma_f32_16x16x32_bf16 v[36:39], v[184:187], v[164:167], v[36:39]\n\t" \
  "ds_read_b128 v[116:119], v96 offset:3200\n\t" \
  "s_waitcnt lgkmcnt(3)\n\t" \
  "v_mfma_f32_16x16x32_bf16 v[40:43], v[184:187], v[168:171], v[40:43]\n\t" \
  "ds_read_b128 v[120:123], v96 offset:2688\n\t" \
  "v_mfma_f32_16x16x32_bf16 v[44:47], v[184:187], v[172:175], v[44:47]\n\t" \
  "ds_read_b128 v[124:127], v96 offset:2176\n\t" \
  "v_mfma_f32_16x16x32_bf16 v[20:23], v[196:199], v[144:147], v[20:23]\n\t" \
  "ds_read_b128 v[128:131], v96 offset:1664\n\t" \
  "v_mfma_f32_16x16x32_bf16 v[24:27], v[196:199], v[148:151], v[24:27]\n\t" \
  "ds_read_b128 v[132:135], v96 offset:1152\n\t" \
  "v_mfma_f32_16x16x32_bf16 v[28:31], v[196:199], v[152:155], v[28:31]\n\t" \
  "ds_read_b128 v[136:139], v96 offset:640\n\t" \
  "v_mfma_f32_16x16x32_bf16 v[32:35], v[196:199], v[156:159], v[32:35]\n\t" \
  "ds_read_b128 v[140:143], v96 offset:128\n\t" \
  "v_mfma_f32_16x16x32_bf16 v[36:39], v[196:199], v[160:163], v[36:39]\n\t" \
  "v_mfma_f32_16x16x32_bf16 v[40:43], v[196:199], v[164:167], v[40:43]\n\t" \
  "v_mfma_f32_16x16x32_bf16 v[44:47], v[196:199], v[168:171], v[44:47]\n\t" \
  "v_mfma_f32_16x16x32_bf16 v[16:19], v[176:179], v[112:115], v[16:19]\n\t" \
  "ds_read2_b32 v[184:185], v97 offset0:48 offset1:49\n\t" \
  "s_waitcnt lgkmcnt(3)\n\t" \
  "v_mfma_f32_16x16x32_bf16 v[20:23], v[176:179], v[116:119], v[20:23]\n\t" \
  "v_perm_b32 v192, v181, v180, v72\n\t" \
  "ds_read2_b32 v[186:187], v97 offset0:50 offset1:51\n\t" \
  "v_mfma_f32_16x16x32_bf16 v[24:27], v[176:179], v[120:123], v[24:27]\n\t" \
  "v_perm_b32 v193, v181, v180, v73\n\t" \
  "ds_read_b128 v[144:147], v96 offset:3776\n\t" \
  "v_mfma_f32_16x16x32_bf16 v[28:31], v[176:179], v[124:127], v[28:31]\n\t" \
  "v_perm_b32 v194, v183, v182, v74\n\t" \
  "ds_read2_b32 v[188:189], v98 offset0:24 offset1:25\n\t" \
  "v_mfma_f32_16x16x32_bf16 v[32:35], v[176:179], v[128:131], v[32:35]\n\t" \
  "v_perm_b32 v195, v183, v182, v75\n\t" \
  "ds_read2_b32 v[190:191], v99 offset0:24 offset1:25\n\t" \
  "v_mfma_f32_16x16x32_bf16 v[36:39], v[176:179], v[132:135], v[36:39]\n\t" \
  "ds_read_b128 v[148:151], v96 offset:3264\n\t" \
  "s_waitcnt lgkmcnt(3)\n\t" \
  "v_mfma_f32_16x16x32_bf16 v[40:43], v[176:179], v[136:139], v[40:43]\n\t" \
  "ds_read_b128 v[152:155], v96 offset:2752\n\t" \
  "v_mfma_f32_16x16x32_bf16 v[44:47], v[176:179], v[140:143], v[44:47]\n\t" \
  "ds_read_b128 v[156:159], v96 offset:2240\n\t" \
  "v_mfma_f32_16x16x32_bf16 v[20:23], v[192:195], v[112:115], v[20:23]\n\t" \
  "ds_read_b128 v[160:163], v96 offset:1728\n\t" \
  "v_mfma_f32_16x16x32_bf16 v[24:27], v[192:195], v[116:119], v[24:27]\n\t" \
  "ds_read_b128 v[164:167], v96 offset:1216\n\t" \
  "v_mfma_f32_16x16x32_bf16 v[28:31], v[192:195], v[120:123], v[28:31]\n\t" \
  "ds_read_b128 v[168:171], v96 offset:704\n\t" \
  "v_mfma_f32_16x16x32_bf16 v[32:35], v[192:195], v[124:127], v[32:35]\n\t" \
  "ds_read_b128 v[172:175], v96 offset:192\n\t" \
  "v_mfma_f32_16x16x32_bf16 v[36:39], v[192:195], v[128:131], v[36:39]\n\t" \
  "v_mfma_f32_16x16x32_bf16 v[40:43], v[192:195], v[132:135], v[40:43]\n\t" \
  "v_mfma_f32_16x16x32_bf16 v[44:47], v[192:195], v[136:139], v[44:47]\n\t" \
  "s_sub_u32 %[n], %[n], 1\n\t" \
  "s_cmp_eq_u32 %[n], 0\n\t" \
  "s_cbranch_scc1 Lcx%=\n\t" \
  "Lcp1%=:\n\t" \
  "s_waitcnt lgkmcnt(8)\n\t" \
  "v_mfma_f32_16x16x32_bf16 v[16:19], v[184:187], v[144:147], v[16:19]\n\t" \
  "ds_read2_b32 v[176:177], v97 offset0:64 offset1:65\n\t" \
  "s_waitcnt lgkmcnt(7)\n\t" \
  "v_mfma_f32_16x16x32_bf16 v[20:23], v[184:187], v[148:151], v[20:23]\n\t" \
  "v_perm_b32 v196, v189, v188, v72\n\t" \
  "ds_read2_b32 v[178:179], v97 offset0:66 offset1:67\n\t" \
  "s_waitcnt lgkmcnt(7)\n\t" \
  "v_mfma_f32_16x16x32_bf16 v[24:27], v[184:187], v[152:155], v[24:27]\n\t" \
  "v_perm_b32 v197, v189, v188, v73\n\t" \
  "ds_read_b128 v[112:115], v96 offset:3840\n\t" \
  "s_waitcnt lgkmcnt(7)\n\t" \
  "v_mfma_f32_16x16x32_bf16 v[28:31], v[184:187], v[156:159], v[28:31]\n\t" \
  "v_perm_b32 v198, v191, v190, v74\n\t" \
  "ds_read2_b32 v[180:181], v98 offset0:32 offset1:33\n\t" \
  "s_waitcnt lgkmcnt(7)\n\t" \
  "v_mfma_f32_16x16x32_bf16 v[32:35], v[184:187], v[160:163], v[32:35]\n\t" \
  "v_perm_b32 v199, v191, v190, v75\n\t" \
  "ds_read2_b32 v[182:183], v99 offset0:32 offset1:33\n\t" \
  "s_waitcnt lgkmcnt(6)\n\t" \
  "v_mfma_f32_16x16x32_bf16 v[36:39], v[184:187], v[164:167], v[36:39]\n\t" \
  "ds_read_b128 v[116:119], v96 offset:3328\n\t" \
  "v_mfma_f32_16x16x32_bf16 v[40:43], v[184:187], v[168:171], v[40:43]\n\t" \
  "ds_read_b128 v[120:123], v96 offset:2816\n\t" \
  "s_waitcnt lgkmcnt(3)\n\t" \
  "v_mfma_f32_16x16x32_bf16 v[44:47], v[184:187], v[172:175], v[44:47]\n\t" \
  "ds_read_b128 v[124:127], v96 offset:2304\n\t" \
  "v_mfma_f32_16x16x32_bf16 v[20:23], v[196:199], v[144:147], v[20:23]\n\t" \
  "ds_read_b128 v[128:131], v96 offset:1792\n\t" \
  "v_mfma_f32_16x16x32_bf16 v[24:27], v[196:199], v[148:151], v[24:27]\n\t" \
  "ds_read_b128 v[132:135], v96 offset:1280\n\t" \
  "v_mfma_f32_16x16x32_bf16 v[28:31], v[196:199], v[152:155], v[28:31]\n\t" \
  "ds_read_b128 v[136:139], v96 offset:768\n\t" \
  "v_mfma_f32_16x16x32_bf16 v[32:35], v[196:199], v[156:159], v[32:35]\n\t" \
  "ds_read_b128 v[140:143], v96 offset:256\n\t" \
  "v_mfma_f32_16x16x32_bf16 v[36:39], v[196:199], v[160:163], v[36:39]\n\t" \
  "v_mfma_f32_16x16x32_bf16 v[40:43], v[196:199], v[164:167], v[40:43]\n\t" \
  "v_mfma_f32_16x16x32_bf16 v[44:47], v[196:199], v[168:171], v[44:47]\n\t" \
  "v_mfma_f32_16x16x32_bf16 v[16:19], v[176:179], v[112:115], v[16:19]\n\t" \
  "ds_read2_b32 v[184:185], v97 offset0:80 offset1:81\n\t" \
  "s_waitcnt lgkmcnt(3)\n\t" \
  "v_mfma_f32_16x16x32_bf16 v[20:23], v[176:179], v[116:119], v[20:23]\n\t" \
  "v_perm_b32 v192, v181, v180, v72\n\t" \
  "ds_read2_b32 v[186:187], v97 offset0:82 offset1:83\n\t" \
  "v_mfma_f32_16x16x32_bf16 v[24:27], v[176:179], v[120:123], v[24:27]\n\t" \
  "v_perm_b32 v193, v181, v180, v73\n\t" \
  "ds_read_b128 v[144:147], v96 offset:3904\n\t" \
  "v_mfma_f32_16x16x32_bf16 v[28:31], v[176:179], v[124:127], v[28:31]\n\t" \
  "v_perm_b32 v194, v183, v182, v74\n\t" \
  "ds_read2_b32 v[188:189], v98 offset0:40 offset1:41\n\t" \
  "v_mfma_f32_16x16x32_bf16 v[32:35], v[176:179], v[128:131], v[32:35]\n\t" \
  "v_perm_b32 v195, v183, v182, v75\n\t" \
  "ds_read2_b32 v[190:191], v99 offset0:40 offset1:41\n\t" \
  "v_mfma_f32_16x16x32_bf16 v[36:39], v[176:179], v[132:135], v[36:39]\n\t" \
  "ds_read_b128 v[148:151], v96 offset:3392\n\t" \
  "s_waitcnt lgkmcnt(3)\n\t" \
  "v_mfma_f32_16x16x32_bf16 v[40:43], v[176:179], v[136:139], v[40:43]\n\t" \
  "ds_read_b128 v[152:155], v96 offset:2880\n\t" \
  "v_mfma_f32_16x16x32_bf16 v[44:47], v[176:179], v[140:143], v[44:47]\n\t" \
  "ds_read_b128 v[156:159], v96 offset:2368\n\t" \
  "v_mfma_f32_16x16x32_bf16 v[20:23], v[192:195], v[112:115], v[20:23]\n\t" \
  "ds_read_b128 v[160:163], v96 offset:1856\n\t" \
  "v_mfma_f32_16x16x32_bf16 v[24:27], v[192:195], v[116:119], v[24:27]\n\t" \
  "ds_read_b128 v[164:167], v96 offset:1344\n\t" \
  "v_mfma_f32_16x16x32_bf16 v[28:31], v[192:195], v[120:123], v[28:31]\n\t" \
  "ds_read_b128 v[168:171], v96 offset:832\n\t" \
  "v_mfma_f32_16x16x32_bf16 v[32:35], v[192:195], v[124:127], v[32:35]\n\t" \
  "ds_read_b128 v[172:175], v96 offset:320\n\t" \
  "v_mfma_f32_16x16x32_bf16 v[36:39], v[192:195], v[128:131], v[36:39]\n\t" \
  "v_mfma_f32_16x16x32_bf16 v[40:43], v[192:195], v[132:135], v[40:43]\n\t" \
  "v_mfma_f32_16x16x32_bf16 v[44:47], v[192:195], v[136:139], v[44:47]\n\t" \
  "v_mfma_f32_16x16x32_bf16 v[16:19], v[184:187], v[144:147], v[16:19]\n\t" \
  "ds_read2_b32 v[176:177], v97 offset0:96 offset1:97\n\t" \
  "s_waitcnt lgkmcnt(3)\n\t" \
  "v_mfma_f32_16x16x32_bf16 v[20:23], v[184:187], v[148:151], v[20:23]\n\t" \
  "v_perm_b32 v196, v189, v188, v72\n\t" \
  "ds_read2_b32 v[178:179], v97 offset0:98 offset1:99\n\t" \
  "v_mfma_f32_16x16x32_bf16 v[24:27], v[184:187], v[152:155], v[24:27]\n\t" \
  "v_perm_b32 v197, v189, v188, v73\n\t" \
  "ds_read_b128 v[112:115], v96 offset:3968\n\t" \
  "v_mfma_f32_16x16x32_bf16 v[28:31], v[184:187], v[156:159], v[28:31]\n\t" \
  "v_perm_b32 v198, v191, v190, v74\n\t" \
  "ds_read2_b32 v[180:181], v98 offset0:48 offset1:49\n\t" \
  "v_mfma_f32_16x16x32_bf16 v[32:35], v[184:187], v[160:163], v[32:35]\n\t" \
  "v_perm_b32 v199, v191, v190, v75\n\t" \
  "ds_read2_b32 v[182:183], v99 offset0:48 offset1:49\n\t" \
  "v_mfma_f32_16x16x32_bf16 v[36:39], v[184:187], v[164:167], v[36:39]\n\t" \
  "ds_read_b128 v[116:119], v96 offset:3456\n\t" \
  "s_waitcnt lgkmcnt(3)\n\t" \
  "v_mfma_f32_16x16x32_bf16 v[40:43], v[184:187], v[168:171], v[40:43]\n\t" \
  "ds_read_b128 v[120:123], v96 offset:2944\n\t" \
  "v_mfma_f32_16x16x32_bf16 v[44:47], v[184:187], v[172:175], v[44:47]\n\t" \
  "ds_read_b128 v[124:127], v96 offset:2432\n\t" \
  "v_mfma_f32_16x16x32_bf16 v[20:23], v[196:199], v[144:147], v[20:23]\n\t" \
  "ds_read_b128 v[128:131], v96 offset:1920\n\t" \
  "v_mfma_f32_16x16x32_bf16 v[24:27], v[196:199], v[148:151], v[24:27]\n\t" \
  "ds_read_b128 v[132:135], v96 offset:1408\n\t" \
  "v_mfma_f32_16x16x32_bf16 v[28:31], v[196:199], v[152:155], v[28:31]\n\t" \
  "ds_read_b128 v[136:139], v96 offset:896\n\t" \
  "v_mfma_f32_16x16x32_bf16 v[32:35], v[196:199], v[156:159], v[32:35]\n\t" \
  "ds_read_b128 v[140:143], v96 offset:384\n\t" \
  "v_mfma_f32_16x16x32_bf16 v[36:39], v[196:199], v[160:163], v[36:39]\n\t" \
  "v_mfma_f32_16x16x32_bf16 v[40:43], v[196:199], v[164:167], v[40:43]\n\t" \
  "v_mfma_f32_16x16x32_bf16 v[44:47], v[196:199], v[168:171], v[44:47]\n\t" \
  "s_sub_u32 %[n], %[n], 1\n\t" \
  "s_cmp_eq_u32 %[n], 0\n\t" \
  "s_cbranch_scc1 Lcx%=\n\t" \
  "Lcp2%=:\n\t" \
  "s_waitcnt lgkmcnt(8)\n\t" \
  "v_mfma_f32_16x16x32_bf16 v[16:19], v[176:179], v[112:115], v[16:19]\n\t" \
  "ds_read2_b32 v[184:185], v97 offset0:112 offset1:113\n\t" \
  "s_waitcnt lgkmcnt(7)\n\t" \
  "v_mfma_f32_16x16x32_bf16 v[20:23], v[176:179], v[116:119], v[20:23]\n\t" \
  "v_perm_b32 v192, v181, v180, v72\n\t" \
  "ds_read2_b32 v[186:187], v97 offset0:114 offset1:115\n\t" \
  "s_waitcnt lgkmcnt(7)\n\t" \
  "v_mfma_f32_16x16x32_bf16 v[24:27], v[176:179], v[120:123], v[24:27]\n\t" \
  "v_perm_b32 v193, v181, v180, v73\n\t" \
  "ds_read_b128 v[144:147], v96 offset:4032\n\t" \
  "s_waitcnt lgkmcnt(7)\n\t" \
  "v_mfma_f32_16x16x32_bf16 v[28:31], v[176:179], v[124:127], v[28:31]\n\t" \
  "v_perm_b32 v194, v183, v182, v74\n\t" \
  "ds_read2_b32 v[188:189], v98 offset0:56 offset1:57\n\t" \
  "s_waitcnt lgkmcnt(7)\n\t" \
  "v_mfma_f32_16x16x32_bf16 v[32:35], v[176:179], v[128:131], v[32:35]\n\t" \
  "v_perm_b32 v195, v183, v182, v75\n\t" \
  "ds_read2_b32 v[190:191], v99 offset0:56 offset1:57\n\t" \
  "s_waitcnt lgkmcnt(6)\n\t" \
  "v_mfma_f32_16x16x32_bf16 v[36:39], v[176:179], v[132:135], v[36:39]\n\t" \
  "ds_read_b128 v[148:151], v96 offset:3520\n\t" \
  "v_mfma_f32_16x16x32_bf16 v[40:43], v[176:179], v[136:139], v[40:43]\n\t" \
  "ds_read_b128 v[152:155], v96 offset:3008\n\t" \
  "s_waitcnt lgkmcnt(3)\n\t" \
  "v_mfma_f32_16x16x32_bf16 v[44:47], v[176:179], v[140:143], v[44:47]\n\t" \
  "ds_read_b128 v[156:159], v96 offset:2496\n\t" \
  "v_mfma_f32_16x16x32_bf16 v[20:23], v[192:195], v[112:115], v[20:23]\n\t" \
  "ds_read_b128 v[160:163], v96 offset:1984\n\t" \
  "v_mfma_f32_16x16x32_bf16 v[24:27], v[192:195], v[116:119], v[24:27]\n\t" \
  "ds_read_b128 v[164:167], v96 offset:1472\n\t" \
  "v_mfma_f32_16x16x32_bf16 v[28:31], v[192:195], v[120:123], v[28:31]\n\t" \
  "ds_read_b128 v[168:171], v96 offset:960\n\t" \
  "v_mfma_f32_16x16x32_bf16 v[32:35], v[192:195], v[124:127], v[32:35]\n\t" \
  "ds_read_b128 v[172:175], v96 offset:448\n\t" \
  "v_mfma_f32_16x16x32_bf16 v[36:39], v[192:195], v[128:131], v[36:39]\n\t" \
  "v_mfma_f32_16x16x32_bf16 v[40:43], v[192:195], v[132:135], v[40:43]\n\t" \
  "v_mfma_f32_16x16x32_bf16 v[44:47], v[192:195], v[136:139], v[44:47]\n\t" \
  "v_mfma_f32_16x16x32_bf16 v[16:19], v[184:187], v[144:147], v[16:19]\n\t" \
  "ds_read_b128 v[112:115], v96 offset:3584\n\t" \
  "s_waitcnt lgkmcnt(3)\n\t" \
  "v_mfma_f32_16x16x32_bf16 v[20:23], v[184:187], v[148:151], v[20:23]\n\t" \
  "v_perm_b32 v196, v189, v188, v72\n\t" \
  "ds_read_b128 v[116:119], v96 offset:3600\n\t" \
  "v_mfma_f32_16x16x32_bf16 v[24:27], v[184:187], v[152:155], v[24:27]\n\t" \
  "v_perm_b32 v197, v189, v188, v73\n\t" \
  "ds_read_b128 v[120:123], v96 offset:3072\n\t" \
  "v_mfma_f32_16x16x32_bf16 v[28:31], v[184:187], v[156:159], v[28:31]\n\t" \
  "v_perm_b32 v198, v191, v190, v74\n\t" \
  "ds_read_b128 v[124:127], v96 offset:3088\n\t" \
  "v_mfma_f32_16x16x32_bf16 v[32:35], v[184:187], v[160:163], v[32:35]\n\t" \
  "v_perm_b32 v199, v191, v190, v75\n\t" \
  "ds_read_b128 v[128:131], v96 offset:2560\n\t" \
  "ds_read_b128 v[132:135], v96 offset:2576\n\t" \
  "v_mfma_f32_16x16x32_bf16 v[36:39], v[184:187], v[164:167], v[36:39]\n\t" \
  "ds_read_b128 v[136:139], v96 offset:2048\n\t" \
  "ds_read_b128 v[140:143], v96 offset:2064\n\t" \
  "s_waitcnt lgkmcnt(5)\n\t" \
  "v_mfma_f32_16x16x32_bf16 v[40:43], v[184:187], v[168:171], v[40:43]\n\t" \
  "ds_read2_b32 v[216:217], v105 offset0:0 offset1:1\n\t" \
  "ds_read2_b32 v[218:219], v105 offset0:2 offset1:3\n\t" \
  "v_mfma_f32_16x16x32_bf16 v[44:47], v[184:187], v[172:175], v[44:47]\n\t" \
  "ds_read2_b32 v[220:221], v105 offset0:16 offset1:17\n\t" \
  "ds_read2_b32 v[222:223], v105 offset0:18 offset1:19\n\t" \
  "v_mfma_f32_16x16x32_bf16 v[20:23], v[196:199], v[144:147], v[20:23]\n\t" \
  "ds_read2_b32 v[224:225], v105 offset0:32 offset1:33\n\t" \
  "ds_read2_b32 v[226:227], v105 offset0:34 offset1:35\n\t" \
  "v_mfma_f32_16x16x32_bf16 v[24:27], v[196:199], v[148:151], v[24:27]\n\t" \
  "ds_read2_b32 v[228:229], v105 offset0:48 offset1:49\n\t" \
  "ds_read2_b32 v[230:231], v105 offset0:50 offset1:51\n\t" \
  "v_mfma_f32_16x16x32_bf16 v[28:31], v[196:199], v[152:155], v[28:31]\n\t" \
  "v_mfma_f32_16x16x32_bf16 v[176:179], v[112:115], v[52:55], 0\n\t" \
  "v_mfma_f32_16x16x32_bf16 v[180:183], v[116:119], v[48:51], 0\n\t" \
  "v_mfma_f32_16x16x32_bf16 v[200:203], v[120:123], v[52:55], 0\n\t" \
  "s_waitcnt lgkmcnt(3)\n\t" \
  "v_mfma_f32_16x16x32_bf16 v[204:207], v[124:127], v[48:51], 0\n\t" \
  "v_mfma_f32_16x16x32_bf16 v[32:35], v[196:199], v[156:159], v[32:35]\n\t" \
  "v_mfma_f32_16x16x32_bf16 v[36:39], v[196:199], v[160:163], v[36:39]\n\t" \
  "s_nop 2\n\t" \
  "v_cvt_pk_bf16_f32 v208, v179, v183\n\t" \
  "v_cvt_pk_bf16_f32 v209, v178, v182\n\t" \
  "v_mfma_f32_16x16x32_bf16 v[40:43], v[196:199], v[164:167], v[40:43]\n\t" \
  "v_cvt_pk_bf16_f32 v210, v177, v181\n\t" \
  "v_cvt_pk_bf16_f32 v211, v176, v180\n\t" \
  "v_mfma_f32_16x16x32_bf16 v[44:47], v[196:199], v[168:171], v[44:47]\n\t" \
  "s_nop 0\n\t" \
  "v_mfma_f32_16x16x32_bf16 v[32:35], v[208:211], v[216:219], v[32:35]\n\t" \
  "v_cvt_pk_bf16_f32 v212, v203, v207\n\t" \
  "v_cvt_pk_bf16_f32 v213, v202, v206\n\t" \
  "ds_read_b128 v[168:171], v96 offset:3584\n\t" \
  "ds_read_b128 v[172:175], v96 offset:3600\n\t" \
  "v_mfma_f32_16x16x32_bf16 v[36:39], v[208:211], v[220:223], v[36:39]\n\t" \
  "v_cvt_pk_bf16_f32 v214, v201, v205\n\t" \
  "v_cvt_pk_bf16_f32 v215, v200, v204\n\t" \
  "ds_read_b128 v[184:187], v96 offset:3072\n\t" \
  "ds_read_b128 v[188:191], v96 offset:3088\n\t" \
  "v_mfma_f32_16x16x32_bf16 v[176:179], v[128:131], v[52:55], 0\n\t" \
  "ds_read_b64 v[144:145], v104 offset:16\n\t" \
  "ds_read_b64 v[146:147], v104 offset:24\n\t" \
  "v_mfma_f32_16x16x32_bf16 v[180:183], v[132:135], v[48:51], 0\n\t" \
  "ds_read_b64 v[148:149], v104 offset:0\n\t" \
  "ds_read_b64 v[150:151], v104 offset:8\n\t" \
  "s_waitcnt lgkmcnt(6)\n\t" \
  "v_mfma_f32_16x16x32_bf16 v[40:43], v[208:211], v[224:227], v[40:43]\n\t" \
  "ds_read_b64 v[152:153], v104 offset:144\n\t" \
  "ds_read_b64 v[154:155], v104 offset:152\n\t" \
  "v_mfma_f32_16x16x32_bf16 v[44:47], v[208:211], v[228:231], v[44:47]\n\t" \
  "ds_read_b64 v[156:157], v104 offset:128\n\t" \
  "ds_read_b64 v[158:159], v104 offset:136\n\t" \
  "v_mfma_f32_16x16x32_bf16 v[200:203], v[136:139], v[52:55], 0\n\t" \
  "ds_read_b64 v[160:161], v104 offset:272\n\t" \
  "ds_read_b64 v[162:163], v104 offset:280\n\t" \
  "v_mfma_f32_16x16x32_bf16 v[204:207], v[140:143], v[48:51], 0\n\t" \
  "ds_read_b64 v[164:165], v104 offset:256\n\t" \
  "ds_read_b64 v[166:167], v104 offset:264\n\t" \
  "v_mfma_f32_16x16x32_bf16 v[36:39], v[212:215], v[216:219], v[36:39]\n\t" \
  "v_cvt_pk_bf16_f32 v208, v179, v183\n\t" \
  "v_cvt_pk_bf16_f32 v209, v178, v182\n\t" \
  "v_mfma_f32_16x16x32_bf16 v[40:43], v[212:215], v[220:223], v[40:43]\n\t" \
  "v_cvt_pk_bf16_f32 v210, v177, v181\n\t" \
  "v_cvt_pk_bf16_f32 v211, v176, v180\n\t" \
  "v_mfma_f32_16x16x32_bf16 v[44:47], v[212:215], v[224:227], v[44:47]\n\t" \
  "s_nop 0\n\t" \
  "v_mfma_f32_16x16x32_bf16 v[40:43], v[208:211], v[216:219], v[40:43]\n\t" \
  "v_cvt_pk_bf16_f32 v212, v203, v207\n\t" \
  "v_cvt_pk_bf16_f32 v213, v202, v206\n\t" \
  "v_mfma_f32_16x16x32_bf16 v[44:47], v[208:211], v[220:223], v[44:47]\n\t" \
  "v_cvt_pk_bf16_f32 v214, v201, v205\n\t" \
  "v_cvt_pk_bf16_f32 v215, v200, v204\n\t" \
  "s_nop 1\n\t" \
  "v_mfma_f32_16x16x32_bf16 v[44:47], v[212:215], v[216:219], v[44:47]\n\t" \
  "s_sub_u32 %[n], %[n], 1\n\t" \
  "s_cmp_eq_u32 %[n], 0\n\t" \
  "s_cbranch_scc1 Lcx%=\n\t" \
  "Lcp3%=:\n\t" \
  "s_waitcnt lgkmcnt(12)\n\t" \
  "v_mfma_f32_16x16x32_bf16 v[116:119], v[168:171], v[60:63], 0\n\t" \
  "ds_read_b128 v[192:195], v96 offset:2560\n\t" \
  "v_mfma_f32_16x16x32_bf16 v[120:123], v[168:171], v[64:67], 0\n\t" \
  "ds_read_b128 v[196:199], v96 offset:2576\n\t" \
  "v_mfma_f32_16x16x32_bf16 v[124:127], v[168:171], v[68:71], 0\n\t" \
  "v_mfma_f32_16x16x32_bf16 v[112:115], v[172:175], v[56:59], 0\n\t" \
  "v_mfma_f32_16x16x32_bf16 v[132:135], v[184:187], v[60:63], 0\n\t" \
  "v_mfma_f32_16x16x32_bf16 v[136:139], v[184:187], v[64:67], 0\n\t" \
  "v_mfma_f32_16x16x32_bf16 v[140:143], v[184:187], v[68:71], 0\n\t" \
  "v_mfma_f32_16x16x32_bf16 v[128:131], v[188:191], v[56:59], 0\n\t" \
  "s_nop 0\n\t" \
  "v_cvt_pk_bf16_f32 v176, v117, v121\n\t" \
  "s_nop 1\n\t" \
  "v_cvt_pk_bf16_f32 v177, v125, v113\n\t" \
  "v_cvt_pk_bf16_f32 v178, v116, v120\n\t" \
  "v_cvt_pk_bf16_f32 v179, v124, v112\n\t" \
  "v_cvt_pk_bf16_f32 v180, v119, v123\n\t" \
  "v_cvt_pk_bf16_f32 v181, v127, v115\n\t" \
  "v_cvt_pk_bf16_f32 v182, v118, v122\n\t" \
  "v_cvt_pk_bf16_f32 v183, v126, v114\n\t" \
  "s_waitcnt lgkmcnt(6)\n\t" \
  "v_mfma_f32_16x16x32_bf16 v[24:27], v[176:179], v[144:147], v[24:27]\n\t" \
  "ds_read_b128 v[168:171], v96 offset:2048\n\t" \
  "v_mfma_f32_16x16x32_bf16 v[28:31], v[176:179], v[152:155], v[28:31]\n\t" \
  "ds_read_b128 v[172:175], v96 offset:2064\n\t" \
  "s_waitcnt lgkmcnt(2)\n\t" \
  "v_mfma_f32_16x16x32_bf16 v[116:119], v[192:195], v[60:63], 0\n\t" \
  "v_mfma_f32_16x16x32_bf16 v[120:123], v[192:195], v[64:67], 0\n\t" \
  "v_mfma_f32_16x16x32_bf16 v[124:127], v[192:195], v[68:71], 0\n\t" \
  "v_mfma_f32_16x16x32_bf16 v[112:115], v[196:199], v[56:59], 0\n\t" \
  "v_cvt_pk_bf16_f32 v200, v133, v137\n\t" \
  "v_cvt_pk_bf16_f32 v201, v141, v129\n\t" \
  "v_mfma_f32_16x16x32_bf16 v[32:35], v[176:179], v[160:163], v[32:35]\n\t" \
  "v_cvt_pk_bf16_f32 v202, v132, v136\n\t" \
  "v_cvt_pk_bf16_f32 v203, v140, v128\n\t" \
  "v_mfma_f32_16x16x32_bf16 v[24:27], v[180:183], v[148:151], v[24:27]\n\t" \
  "v_cvt_pk_bf16_f32 v204, v135, v139\n\t" \
  "v_cvt_pk_bf16_f32 v205, v143, v131\n\t" \
  "v_mfma_f32_16x16x32_bf16 v[28:31], v[180:183], v[156:159], v[28:31]\n\t" \
  "v_cvt_pk_bf16_f32 v206, v134, v138\n\t" \
  "v_cvt_pk_bf16_f32 v207, v142, v130\n\t" \
  "v_mfma_f32_16x16x32_bf16 v[32:35], v[180:183], v[164:167], v[32:35]\n\t" \
  "v_mfma_f32_16x16x32_bf16 v[28:31], v[200:203], v[144:147], v[28:31]\n\t" \
  "ds_read_b128 v[184:187], v96 offset:1536\n\t" \
  "v_mfma_f32_16x16x32_bf16 v[32:35], v[200:203], v[152:155], v[32:35]\n\t" \
  "ds_read_b128 v[188:191], v96 offset:1552\n\t" \
  "s_waitcnt lgkmcnt(2)\n\t" \
  "v_mfma_f32_16x16x32_bf16 v[132:135], v[168:171], v[60:63], 0\n\t" \
  "v_mfma_f32_16x16x32_bf16 v[136:139], v[168:171], v[64:67], 0\n\t" \
  "v_mfma_f32_16x16x32_bf16 v[140:143], v[168:171], v[68:71], 0\n\t" \
  "v_mfma_f32_16x16x32_bf16 v[128:131], v[172:175], v[56:59], 0\n\t" \
  "v_cvt_pk_bf16_f32 v176, v117, v121\n\t" \
  "v_cvt_pk_bf16_f32 v177, v125, v113\n\t" \
  "v_mfma_f32_16x16x32_bf16 v[36:39], v[200:203], v[160:163], v[36:39]\n\t" \
  "v_cvt_pk_bf16_f32 v178, v116, v120\n\t" \
  "v_cvt_pk_bf16_f32 v179, v124, v112\n\t" \
  "v_mfma_f32_16x16x32_bf16 v[28:31], v[204:207], v[148:151], v[28:31]\n\t" \
  "v_cvt_pk_bf16_f32 v180, v119, v123\n\t" \
  "v_cvt_pk_bf16_f32 v181, v127, v115\n\t" \
  "v_mfma_f32_16x16x32_bf16 v[32:35], v[204:207], v[156:159], v[32:35]\n\t" \
  "v_cvt_pk_bf16_f32 v182, v118, v122\n\t" \
  "v_cvt_pk_bf16_f32 v183, v126, v114\n\t" \
  "v_mfma_f32_16x16x32_bf16 v[36:39], v[204:207], v[164:167], v[36:39]\n\t" \
  "v_mfma_f32_16x16x32_bf16 v[32:35], v[176:179], v[144:147], v[32:35]\n\t" \
  "ds_read_b128 v[192:195], v96 offset:1024\n\t" \
  "v_mfma_f32_16x16x32_bf16 v[36:39], v[176:179], v[152:155], v[36:39]\n\t" \
  "ds_read_b128 v[196:199], v96 offset:1040\n\t" \
  "s_waitcnt lgkmcnt(2)\n\t" \
  "v_mfma_f32_16x16x32_bf16 v[116:119], v[184:187], v[60:63], 0\n\t" \
  "v_mfma_f32_16x16x32_bf16 v[120:123], v[184:187], v[64:67], 0\n\t" \
  "v_mfma_f32_16x16x32_bf16 v[124:127], v[184:187], v[68:71], 0\n\t" \
  "v_mfma_f32_16x16x32_bf16 v[112:115], v[188:191], v[56:59], 0\n\t" \
  "v_cvt_pk_bf16_f32 v200, v133, v137\n\t" \
  "v_cvt_pk_bf16_f32 v201, v141, v129\n\t" \
  "v_mfma_f32_16x16x32_bf16 v[40:43], v[176:179], v[160:163], v[40:43]\n\t" \
  "v_cvt_pk_bf16_f32 v202, v132, v136\n\t" \
  "v_cvt_pk_bf16_f32 v203, v140, v128\n\t" \
  "v_mfma_f32_16x16x32_bf16 v[32:35], v[180:183], v[148:151], v[32:35]\n\t" \
  "v_cvt_pk_bf16_f32 v204, v135, v139\n\t" \
  "v_cvt_pk_bf16_f32 v205, v143, v131\n\t" \
  "v_mfma_f32_16x16x32_bf16 v[36:39], v[180:183], v[156:159], v[36:39]\n\t" \
  "v_cvt_pk_bf16_f32 v206, v134, v138\n\t" \
  "v_cvt_pk_bf16_f32 v207, v142, v130\n\t" \
  "v_mfma_f32_16x16x32_bf16 v[40:43], v[180:183], v[164:167], v[40:43]\n\t" \
  "v_mfma_f32_16x16x32_bf16 v[36:39], v[200:203], v[144:147], v[36:39]\n\t" \
  "v_mfma_f32_16x16x32_bf16 v[40:43], v[200:203], v[152:155], v[40:43]\n\t" \
  "s_waitcnt lgkmcnt(0)\n\t" \
  "v_mfma_f32_16x16x32_bf16 v[132:135], v[192:195], v[60:63], 0\n\t" \
  "v_mfma_f32_16x16x32_bf16 v[136:139], v[192:195], v[64:67], 0\n\t" \
  "v_mfma_f32_16x16x32_bf16 v[140:143], v[192:195], v[68:71], 0\n\t" \
  "v_mfma_f32_16x16x32_bf16 v[128:131], v[196:199], v[56:59], 0\n\t" \
  "v_cvt_pk_bf16_f32 v176, v117, v121\n\t" \
  "v_cvt_pk_bf16_f32 v177, v125, v113\n\t" \
  "v_mfma_f32_16x16x32_bf16 v[44:47], v[200:203], v[160:163], v[44:47]\n\t" \
  "v_cvt_pk_bf16_f32 v178, v116, v120\n\t" \
  "v_cvt_pk_bf16_f32 v179, v124, v112\n\t" \
  "v_mfma_f32_16x16x32_bf16 v[36:39], v[204:207], v[148:151], v[36:39]\n\t" \
  "v_cvt_pk_bf16_f32 v180, v119, v123\n\t" \
  "v_cvt_pk_bf16_f32 v181, v127, v115\n\t" \
  "v_mfma_f32_16x16x32_bf16 v[40:43], v[204:207], v[156:159], v[40:43]\n\t" \
  "v_cvt_pk_bf16_f32 v182, v118, v122\n\t" \
  "v_cvt_pk_bf16_f32 v183, v126, v114\n\t" \
  "v_mfma_f32_16x16x32_bf16 v[44:47], v[204:207], v[164:167], v[44:47]\n\t" \
  "v_mfma_f32_16x16x32_bf16 v[40:43], v[176:179], v[144:147], v[40:43]\n\t" \
  "v_cvt_pk_bf16_f32 v200, v133, v137\n\t" \
  "v_cvt_pk_bf16_f32 v201, v141, v129\n\t" \
  "v_mfma_f32_16x16x32_bf16 v[44:47], v[176:179], v[152:155], v[44:47]\n\t" \
  "v_cvt_pk_bf16_f32 v202, v132, v136\n\t" \
  "v_cvt_pk_bf16_f32 v203, v140, v128\n\t" \
  "v_mfma_f32_16x16x32_bf16 v[40:43], v[180:183], v[148:151], v[40:43]\n\t" \
  "v_cvt_pk_bf16_f32 v204, v135, v139\n\t" \
  "v_cvt_pk_bf16_f32 v205, v143, v131\n\t" \
  "v_mfma_f32_16x16x32_bf16 v[44:47], v[180:183], v[156:159], v[44:47]\n\t" \
  "v_cvt_pk_bf16_f32 v206, v134, v138\n\t" \
  "v_cvt_pk_bf16_f32 v207, v142, v130\n\t" \
  "v_mfma_f32_16x16x32_bf16 v[44:47], v[200:203], v[144:147], v[44:47]\n\t" \
  "s_nop 0\n\t" \
  "v_mfma_f32_16x16x32_bf16 v[44:47], v[204:207], v[148:151], v[44:47]\n\t" \
  "s_branch Lcx%=\n\t" \
  "Lce%=:\n\t" \
  "s_cmp_lg_u32 %[p], 1\n\t" \
  "s_cbranch_scc1 Lce1%=\n\t" \
  "ds_read2_b32 v[184:185], v97 offset0:48 offset1:49\n\t" \
  "ds_read2_b32 v[186:187], v97 offset0:50 offset1:51\n\t" \
  "ds_read_b128 v[144:147], v96 offset:3776\n\t" \
  "ds_read2_b32 v[188:189], v98 offset0:24 offset1:25\n\t" \
  "ds_read2_b32 v[190:191], v99 offset0:24 offset1:25\n\t" \
  "ds_read_b128 v[148:151], v96 offset:3264\n\t" \
  "ds_read_b128 v[152:155], v96 offset:2752\n\t" \
  "ds_read_b128 v[156:159], v96 offset:2240\n\t" \
  "ds_read_b128 v[160:163], v96 offset:1728\n\t" \
  "ds_read_b128 v[164:167], v96 offset:1216\n\t" \
  "ds_read_b128 v[168:171], v96 offset:704\n\t" \
  "ds_read_b128 v[172:175], v96 offset:192\n\t" \
  "s_branch Lcp1%=\n\t" \
  "Lce1%=:\n\t" \
  "s_cmp_lg_u32 %[p], 2\n\t" \
  "s_cbranch_scc1 Lce2%=\n\t" \
  "ds_read2_b32 v[176:177], v97 offset0:96 offset1:97\n\t" \
  "ds_read2_b32 v[178:179], v97 offset0:98 offset1:99\n\t" \
  "ds_read_b128 v[112:115], v96 offset:3968\n\t" \
  "ds_read2_b32 v[180:181], v98 offset0:48 offset1:49\n\t" \
  "ds_read2_b32 v[182:183], v99 offset0:48 offset1:49\n\t" \
  "ds_read_b128 v[116:119], v96 offset:3456\n\t" \
  "ds_read_b128 v[120:123], v96 offset:2944\n\t" \
  "ds_read_b128 v[124:127], v96 offset:2432\n\t" \
  "ds_read_b128 v[128:131], v96 offset:1920\n\t" \
  "ds_read_b128 v[132:135], v96 offset:1408\n\t" \
  "ds_read_b128 v[136:139], v96 offset:896\n\t" \
  "ds_read_b128 v[140:143], v96 offset:384\n\t" \
  "s_branch Lcp2%=\n\t" \
  "Lce2%=:\n\t" \
  "ds_read_b128 v[168:171], v96 offset:3584\n\t" \
  "ds_read_b128 v[172:175], v96 offset:3600\n\t" \
  "ds_read_b128 v[184:187], v96 offset:3072\n\t" \
  "ds_read_b128 v[188:191], v96 offset:3088\n\t" \
  "ds_read_b64 v[144:145], v104 offset:16\n\t" \
  "ds_read_b64 v[146:147], v104 offset:24\n\t" \
  "ds_read_b64 v[148:149], v104 offset:0\n\t" \
  "ds_read_b64 v[150:151], v104 offset:8\n\t" \
  "ds_read_b64 v[152:153], v104 offset:144\n\t" \
  "ds_read_b64 v[154:155], v104 offset:152\n\t" \
  "ds_read_b64 v[156:157], v104 offset:128\n\t" \
  "ds_read_b64 v[158:159], v104 offset:136\n\t" \
  "ds_read_b64 v[160:161], v104 offset:272\n\t" \
  "ds_read_b64 v[162:163], v104 offset:280\n\t" \
  "ds_read_b64 v[164:165], v104 offset:256\n\t" \
  "ds_read_b64 v[166:167], v104 offset:264\n\t" \
  "s_branch Lcp3%=\n\t" \
  "Lcx%=:\n\t" \
  "s_waitcnt lgkmcnt(0)\n\t" \
  "s_nop 7\n\t"
#define CONV_CLOB "v112", "v113", "v114", "v115", "v116", "v117", "v118", "v119", "v120", "v121", "v122", "v123", "v124", "v125", "v126", "v127", "v128", "v129", "v130", "v131", "v132", "v133", "v134", "v135", "v136", "v137", "v138", "v139", "v140", "v141", "v142", "v143", "v144", "v145", "v146", "v147", "v148", "v149", "v150", "v151", "v152", "v153", "v154", "v155", "v156", "v157", "v158", "v159", "v160", "v161", "v162", "v163", "v164", "v165", "v166", "v167", "v168", "v169", "v170", "v171", "v172", "v173", "v174", "v175", "v176", "v177", "v178", "v179", "v180", "v181", "v182", "v183", "v184", "v185", "v186", "v187", "v188", "v189", "v190", "v191", "v192", "v193", "v194", "v195", "v196", "v197", "v198", "v199", "v200", "v201", "v202", "v203", "v204", "v205", "v206", "v207", "v208", "v209", "v210", "v211", "v212", "v213", "v214", "v215", "v216", "v217", "v218", "v219", "v220", "v221", "v222", "v223", "v224", "v225", "v226", "v227", "v228", "v229", "v230", "v231"

#define GEN_ASM \
  "v_mov_b32 v32, 0\n\t" \
  "v_mov_b32 v33, 0\n\t" \
  "v_mov_b32 v34, 0\n\t" \
  "v_mov_b32 v35, 0\n\t" \
  "v_mov_b32 v36, 0\n\t" \
  "v_mov_b32 v37, 0\n\t" \
  "ds_read_u16_d16_hi v32, %[pa] offset:0\n\t" \
  "ds_read_u16_d16_hi v33, %[pa] offset:128\n\t" \
  "ds_read_u16_d16_hi v34, %[pa] offset:256\n\t" \
  "ds_read_u16_d16_hi v35, %[pa] offset:384\n\t" \
  "ds_read_u16_d16_hi v36, %[pa] offset:512\n\t" \
  "ds_read_u16_d16_hi v37, %[pa] offset:640\n\t" \
  "s_waitcnt lgkmcnt(0)\n\t" \
  "v_mul_f32 v38, %[dl], v32\n\t" \
  "v_mul_f32 v39, %[dr], v33\n\t" \
  "v_mul_f32 v43, v37, v35\n\t" \
  "v_mul_f32 v44, v36, v35\n\t" \
  "v_fma_f32 v40, v36, v34, -v43\n\t" \
  "v_fma_f32 v41, -v37, v34, -v44\n\t" \
  "v_mul_f32 v45, 0x41000000, v38\n\t" \
  "v_max_f32_e64 v42, |v40|, |v41|\n\t" \
  "v_mul_f32 v46, 0xc1000000, v39\n\t" \
  "v_exp_f32 v45, v45\n\t" \
  "v_fract_f32 v46, v46\n\t" \
  "v_max_u32_dpp v42, v42, v42 row_shr:1 row_mask:0xf bank_mask:0xf bound_ctrl:1\n\t" \
  "v_cos_f32 v47, v46\n\t" \
  "v_sin_f32 v48, v46\n\t" \
  "v_max_u32_dpp v42, v42, v42 row_shr:2 row_mask:0xf bank_mask:0xf bound_ctrl:1\n\t" \
  "v_exp_f32 v49, v38\n\t" \
  "v_mul_f32 v47, v45, v47\n\t" \
  "v_mul_f32 v48, v45, v48\n\t" \
  "v_max_u32_dpp v42, v42, v42 row_shr:4 row_mask:0xf bank_mask:0xf bound_ctrl:1\n\t" \
  "v_fract_f32 v50, v39\n\t" \
  "v_mul_f32 v53, 0x43000000, v38\n\t" \
  "v_max_u32_dpp v42, v42, v42 row_shr:8 row_mask:0xf bank_mask:0xf bound_ctrl:1\n\t" \
  "v_cos_f32 v51, v50\n\t" \
  "v_sin_f32 v52, v50\n\t" \
  "v_max_u32_dpp v42, v42, v42 row_bcast:15 row_mask:0xa bank_mask:0xf\n\t" \
  "v_mul_f32 v54, 0x43000000, v39\n\t" \
  "v_mul_f32 v51, v49, v51\n\t" \
  "v_max_u32_dpp v42, v42, v42 row_bcast:31 row_mask:0xc bank_mask:0xf\n\t" \
  "v_mul_f32 v52, v49, v52\n\t" \
  "v_exp_f32 v53, v53\n\t" \
  "v_readlane_b32 %[sx], v42, 63\n\t" \
  "v_fract_f32 v54, v54\n\t" \
  "v_cvt_pk_f16_f32 v57, v47, v48\n\t" \
  "s_and_b32 %[sx], %[sx], 0x7f800000\n\t" \
  "s_max_u32 %[sx], %[sx], 0x01000000\n\t" \
  "s_min_u32 %[sx], %[sx], 0x7e000000\n\t" \
  "s_sub_u32 %[sy], 0x7f000000, %[sx]\n\t" \
  "v_cos_f32 v55, v54\n\t" \
  "v_sin_f32 v56, v54\n\t" \
  "v_mul_f32 v40, %[sy], v40\n\t" \
  "v_mul_f32 v41, %[sy], v41\n\t" \
  "v_mul_f32 v55, v53, v55\n\t" \
  "v_mul_f32 v56, v53, v56\n\t" \
  "v_cvt_pk_f16_f32 v58, v40, v41\n\t" \
  "v_cvt_pk_f16_f32 v59, v51, v52\n\t" \
  "v_cvt_pk_f16_f32 v60, v55, v56\n\t" \
  "v_mul_f32 v45, v48, v48\n\t" \
  "v_add_f32 v46, v47, v47\n\t" \
  "v_fma_f32 v45, v47, v47, -v45\n\t" \
  "v_mul_f32 v46, v46, v48\n\t" \
  "v_mul_f32 v49, v52, v52\n\t" \
  "v_cvt_pk_f16_f32 v61, v45, v46\n\t" \
  "v_add_f32 v50, v51, v51\n\t" \
  "v_fma_f32 v49, v51, v51, -v49\n\t" \
  "v_mul_f32 v50, v50, v52\n\t" \
  "v_mul_f32 v53, v56, v56\n\t" \
  "v_cvt_pk_f16_f32 v62, v49, v50\n\t" \
  "v_add_f32 v54, v55, v55\n\t" \
  "v_fma_f32 v53, v55, v55, -v53\n\t" \
  "v_mul_f32 v54, v54, v56\n\t" \
  "v_pk_mul_f16 v64, v58, v57 op_sel_hi:[0,1]\n\t" \
  "v_cvt_pk_f16_f32 v63, v53, v54\n\t" \
  "ds_write_addtid_b32 v58 offset:0\n\t" \
  "v_pk_fma_f16 v64, v58, v57, v64 op_sel:[1,1,0] op_sel_hi:[1,0,1] neg_lo:[0,1,0]\n\t" \
  "v_mul_f32 v47, v46, v46\n\t" \
  "v_add_f32 v48, v45, v45\n\t" \
  "v_fma_f32 v47, v45, v45, -v47\n\t" \
  "v_mul_f32 v48, v48, v46\n\t" \
  "v_mul_f32 v51, v50, v50\n\t" \
  "v_cvt_pk_f16_f32 v65, v47, v48\n\t" \
  "v_add_f32 v52, v49, v49\n\t" \
  "v_fma_f32 v51, v49, v49, -v51\n\t" \
  "v_mul_f32 v52, v52, v50\n\t" \
  "v_pk_mul_f16 v67, v58, v61 op_sel_hi:[0,1]\n\t" \
  "v_cvt_pk_f16_f32 v66, v51, v52\n\t" \
  "v_pk_mul_f16 v68, v64, v61 op_sel_hi:[0,1]\n\t" \
  "v_pk_fma_f16 v67, v58, v61, v67 op_sel:[1,1,0] op_sel_hi:[1,0,1] neg_lo:[0,1,0]\n\t" \
  "ds_write_addtid_b32 v64 offset:288\n\t" \
  "v_pk_fma_f16 v68, v64, v61, v68 op_sel:[1,1,0] op_sel_hi:[1,0,1] neg_lo:[0,1,0]\n\t" \
  "v_mul_f32 v45, v48, v48\n\t" \
  "v_add_f32 v46, v47, v47\n\t" \
  "v_fma_f32 v45, v47, v47, -v45\n\t" \
  "v_mul_f32 v46, v46, v48\n\t" \
  "v_mul_f32 v55, v54, v54\n\t" \
  "v_cvt_pk_f16_f32 v69, v45, v46\n\t" \
  "v_add_f32 v56, v53, v53\n\t" \
  "v_fma_f32 v55, v53, v53, -v55\n\t" \
  "v_mul_f32 v56, v56, v54\n\t" \
  "v_pk_mul_f16 v71, v58, v65 op_sel_hi:[0,1]\n\t" \
  "v_cvt_pk_f16_f32 v70, v55, v56\n\t" \
  "v_pk_mul_f16 v72, v64, v65 op_sel_hi:[0,1]\n\t" \
  "v_pk_fma_f16 v71, v58, v65, v71 op_sel:[1,1,0] op_sel_hi:[1,0,1] neg_lo:[0,1,0]\n\t" \
  "v_pk_mul_f16 v73, v67, v65 op_sel_hi:[0,1]\n\t" \
  "v_pk_fma_f16 v72, v64, v65, v72 op_sel:[1,1,0] op_sel_hi:[1,0,1] neg_lo:[0,1,0]\n\t" \
  "v_pk_mul_f16 v74, v68, v65 op_sel_hi:[0,1]\n\t" \
  "v_pk_fma_f16 v73, v67, v65, v73 op_sel:[1,1,0] op_sel_hi:[1,0,1] neg_lo:[0,1,0]\n\t" \
  "ds_write_addtid_b32 v67 offset:576\n\t" \
  "v_pk_fma_f16 v74, v68, v65, v74 op_sel:[1,1,0] op_sel_hi:[1,0,1] neg_lo:[0,1,0]\n\t" \
  "ds_write_addtid_b32 v68 offset:864\n\t" \
  "v_pk_mul_f16 v75, v58, v69 op_sel_hi:[0,1]\n\t" \
  "ds_write_addtid_b32 v71 offset:1152\n\t" \
  "v_pk_mul_f16 v76, v64, v69 op_sel_hi:[0,1]\n\t" \
  "v_pk_fma_f16 v75, v58, v69, v75 op_sel:[1,1,0] op_sel_hi:[1,0,1] neg_lo:[0,1,0]\n\t" \
  "ds_write_addtid_b32 v72 offset:1440\n\t" \
  "v_pk_fma_f16 v76, v64, v69, v76 op_sel:[1,1,0] op_sel_hi:[1,0,1] neg_lo:[0,1,0]\n\t" \
  "v_pk_mul_f16 v77, v67, v69 op_sel_hi:[0,1]\n\t" \
  "ds_write_addtid_b32 v73 offset:1728\n\t" \
  "v_pk_mul_f16 v78, v68, v69 op_sel_hi:[0,1]\n\t" \
  "v_pk_fma_f16 v77, v67, v69, v77 op_sel:[1,1,0] op_sel_hi:[1,0,1] neg_lo:[0,1,0]\n\t" \
  "ds_write_addtid_b32 v74 offset:2016\n\t" \
  "v_pk_fma_f16 v78, v68, v69, v78 op_sel:[1,1,0] op_sel_hi:[1,0,1] neg_lo:[0,1,0]\n\t" \
  "v_pk_mul_f16 v79, v71, v69 op_sel_hi:[0,1]\n\t" \
  "v_pk_mul_f16 v80, v72, v69 op_sel_hi:[0,1]\n\t" \
  "v_pk_mul_f16 v81, v73, v69 op_sel_hi:[0,1]\n\t" \
  "v_pk_fma_f16 v79, v71, v69, v79 op_sel:[1,1,0] op_sel_hi:[1,0,1] neg_lo:[0,1,0]\n\t" \
  "v_pk_fma_f16 v80, v72, v69, v80 op_sel:[1,1,0] op_sel_hi:[1,0,1] neg_lo:[0,1,0]\n\t" \
  "v_pk_fma_f16 v81, v73, v69, v81 op_sel:[1,1,0] op_sel_hi:[1,0,1] neg_lo:[0,1,0]\n\t" \
  "v_pk_mul_f16 v82, v74, v69 op_sel_hi:[0,1]\n\t" \
  "v_mov_b32 v83, 0x3c00\n\t" \
  "v_pk_mul_f16 v84, v59, v62 op_sel_hi:[0,1]\n\t" \
  "v_pk_fma_f16 v82, v74, v69, v82 op_sel:[1,1,0] op_sel_hi:[1,0,1] neg_lo:[0,1,0]\n\t" \
  "v_mul_f32 v53, v56, v56\n\t" \
  "v_pk_fma_f16 v84, v59, v62, v84 op_sel:[1,1,0] op_sel_hi:[1,0,1] neg_lo:[0,1,0]\n\t" \
  "v_add_f32 v54, v55, v55\n\t" \
  "v_fma_f32 v53, v55, v55, -v53\n\t" \
  "v_mul_f32 v54, v54, v56\n\t" \
  "ds_write_addtid_b32 v75 offset:2304\n\t" \
  "v_cvt_pk_f16_f32 v85, v53, v54\n\t" \
  "ds_write_addtid_b32 v76 offset:2592\n\t" \
  "ds_write_addtid_b32 v77 offset:2880\n\t" \
  "ds_write_addtid_b32 v78 offset:3168\n\t" \
  "v_pk_mul_f16 v86, v59, v66 op_sel_hi:[0,1]\n\t" \
  "v_pk_mul_f16 v87, v62, v66 op_sel_hi:[0,1]\n\t" \
  "v_pk_mul_f16 v88, v84, v66 op_sel_hi:[0,1]\n\t" \
  "v_pk_fma_f16 v86, v59, v66, v86 op_sel:[1,1,0] op_sel_hi:[1,0,1] neg_lo:[0,1,0]\n\t" \
  "v_pk_fma_f16 v87, v62, v66, v87 op_sel:[1,1,0] op_sel_hi:[1,0,1] neg_lo:[0,1,0]\n\t" \
  "v_pk_fma_f16 v88, v84, v66, v88 op_sel:[1,1,0] op_sel_hi:[1,0,1] neg_lo:[0,1,0]\n\t" \
  "v_pk_mul_f16 v89, v60, v63 op_sel_hi:[0,1]\n\t" \
  "ds_write_addtid_b32 v79 offset:3456\n\t" \
  "ds_write_addtid_b32 v80 offset:3744\n\t" \
  "v_pk_fma_f16 v89, v60, v63, v89 op_sel:[1,1,0] op_sel_hi:[1,0,1] neg_lo:[0,1,0]\n\t" \
  "ds_write_addtid_b32 v81 offset:4032\n\t" \
  "ds_write_addtid_b32 v82 offset:4320\n\t" \
  "ds_read_b128 v[90:93], %[sr] offset:0\n\t" \
  "ds_read_b128 v[94:97], %[sr] offset:64\n\t" \
  "ds_read_b128 v[98:101], %[sr] offset:128\n\t" \
  "ds_read_b128 v[102:105], %[sr] offset:192\n\t" \
  "v_mul_f32 v55, v54, v54\n\t" \
  "v_add_f32 v56, v53, v53\n\t" \
  "v_fma_f32 v55, v53, v53, -v55\n\t" \
  "v_mul_f32 v56, v56, v54\n\t" \
  "v_pk_mul_f16 v107, v59, v60 op_sel_hi:[0,1]\n\t" \
  "v_cvt_pk_f16_f32 v106, v55, v56\n\t" \
  "ds_write_addtid_b32 v83 offset:0\n\t" \
  "v_pk_fma_f16 v107, v59, v60, v107 op_sel:[1,1,0] op_sel_hi:[1,0,1] neg_lo:[0,1,0]\n\t" \
  "v_pk_mul_f16 v108, v62, v60 op_sel_hi:[0,1]\n\t" \
  "ds_write_addtid_b32 v59 offset:288\n\t" \
  "v_pk_mul_f16 v109, v84, v60 op_sel_hi:[0,1]\n\t" \
  "v_pk_fma_f16 v108, v62, v60, v108 op_sel:[1,1,0] op_sel_hi:[1,0,1] neg_lo:[0,1,0]\n\t" \
  "ds_write_addtid_b32 v62 offset:576\n\t" \
  "v_pk_fma_f16 v109, v84, v60, v109 op_sel:[1,1,0] op_sel_hi:[1,0,1] neg_lo:[0,1,0]\n\t" \
  "v_pk_mul_f16 v110, v66, v60 op_sel_hi:[0,1]\n\t" \
  "ds_write_addtid_b32 v84 offset:864\n\t" \
  "v_pk_mul_f16 v111, v86, v60 op_sel_hi:[0,1]\n\t" \
  "v_pk_fma_f16 v110, v66, v60, v110 op_sel:[1,1,0] op_sel_hi:[1,0,1] neg_lo:[0,1,0]\n\t" \
  "ds_write_addtid_b32 v66 offset:1152\n\t" \
  "v_pk_fma_f16 v111, v86, v60, v111 op_sel:[1,1,0] op_sel_hi:[1,0,1] neg_lo:[0,1,0]\n\t" \
  "v_pk_mul_f16 v112, v87, v60 op_sel_hi:[0,1]\n\t" \
  "ds_write_addtid_b32 v86 offset:1440\n\t" \
  "v_pk_mul_f16 v113, v88, v60 op_sel_hi:[0,1]\n\t" \
  "v_pk_fma_f16 v112, v87, v60, v112 op_sel:[1,1,0] op_sel_hi:[1,0,1] neg_lo:[0,1,0]\n\t" \
  "ds_write_addtid_b32 v87 offset:1728\n\t" \
  "v_pk_fma_f16 v113, v88, v60, v113 op_sel:[1,1,0] op_sel_hi:[1,0,1] neg_lo:[0,1,0]\n\t" \
  "ds_write_addtid_b32 v88 offset:2016\n\t" \
  "ds_write_addtid_b32 v60 offset:2304\n\t" \
  "v_pk_mul_f16 v114, v60, v70 op_sel_hi:[0,1]\n\t" \
  "v_pk_mul_f16 v115, v63, v70 op_sel_hi:[0,1]\n\t" \
  "v_pk_mul_f16 v116, v89, v70 op_sel_hi:[0,1]\n\t" \
  "v_pk_fma_f16 v114, v60, v70, v114 op_sel:[1,1,0] op_sel_hi:[1,0,1] neg_lo:[0,1,0]\n\t" \
  "v_pk_fma_f16 v115, v63, v70, v115 op_sel:[1,1,0] op_sel_hi:[1,0,1] neg_lo:[0,1,0]\n\t" \
  "v_pk_fma_f16 v116, v89, v70, v116 op_sel:[1,1,0] op_sel_hi:[1,0,1] neg_lo:[0,1,0]\n\t" \
  "ds_write_addtid_b32 v107 offset:2592\n\t" \
  "ds_write_addtid_b32 v108 offset:2880\n\t" \
  "ds_write_addtid_b32 v109 offset:3168\n\t" \
  "v_pk_mul_f16 v117, v63, v62 op_sel_hi:[0,1]\n\t" \
  "ds_write_addtid_b32 v110 offset:3456\n\t" \
  "v_pk_mul_f16 v118, v63, v66 op_sel_hi:[0,1]\n\t" \
  "v_pk_fma_f16 v117, v63, v62, v117 op_sel:[1,1,0] op_sel_hi:[1,0,1] neg_lo:[0,1,0]\n\t" \
  "ds_write_addtid_b32 v111 offset:3744\n\t" \
  "v_pk_fma_f16 v118, v63, v66, v118 op_sel:[1,1,0] op_sel_hi:[1,0,1] neg_lo:[0,1,0]\n\t" \
  "v_pk_mul_f16 v119, v63, v87 op_sel_hi:[0,1]\n\t" \
  "ds_write_addtid_b32 v112 offset:4032\n\t" \
  "v_pk_mul_f16 v120, v89, v62 op_sel_hi:[0,1]\n\t" \
  "v_pk_fma_f16 v119, v63, v87, v119 op_sel:[1,1,0] op_sel_hi:[1,0,1] neg_lo:[0,1,0]\n\t" \
  "ds_write_addtid_b32 v113 offset:4320\n\t" \
  "v_pk_fma_f16 v120, v89, v62, v120 op_sel:[1,1,0] op_sel_hi:[1,0,1] neg_lo:[0,1,0]\n\t" \
  "ds_read_b128 v[122:125], %[sr] offset:0\n\t" \
  "ds_read_b128 v[126:129], %[sr] offset:64\n\t" \
  "ds_read_b128 v[130:133], %[sr] offset:128\n\t" \
  "ds_read_b128 v[134:137], %[sr] offset:192\n\t" \
  "v_pk_mul_f16 v138, v89, v66 op_sel_hi:[0,1]\n\t" \
  "v_pk_mul_f16 v139, v89, v87 op_sel_hi:[0,1]\n\t" \
  "v_pk_mul_f16 v140, v70, v66 op_sel_hi:[0,1]\n\t" \
  "v_pk_fma_f16 v138, v89, v66, v138 op_sel:[1,1,0] op_sel_hi:[1,0,1] neg_lo:[0,1,0]\n\t" \
  "v_pk_fma_f16 v139, v89, v87, v139 op_sel:[1,1,0] op_sel_hi:[1,0,1] neg_lo:[0,1,0]\n\t" \
  "v_pk_fma_f16 v140, v70, v66, v140 op_sel:[1,1,0] op_sel_hi:[1,0,1] neg_lo:[0,1,0]\n\t" \
  "ds_write_addtid_b32 v63 offset:0\n\t" \
  "ds_write_addtid_b32 v117 offset:288\n\t" \
  "v_pk_mul_f16 v141, v114, v66 op_sel_hi:[0,1]\n\t" \
  "ds_write_addtid_b32 v118 offset:576\n\t" \
  "ds_write_addtid_b32 v119 offset:864\n\t" \
  "v_pk_fma_f16 v141, v114, v66, v141 op_sel:[1,1,0] op_sel_hi:[1,0,1] neg_lo:[0,1,0]\n\t" \
  "v_pk_mul_f16 v142, v115, v66 op_sel_hi:[0,1]\n\t" \
  "ds_write_addtid_b32 v89 offset:1152\n\t" \
  "ds_write_addtid_b32 v120 offset:1440\n\t" \
  "v_pk_fma_f16 v142, v115, v66, v142 op_sel:[1,1,0] op_sel_hi:[1,0,1] neg_lo:[0,1,0]\n\t" \
  "v_pk_mul_f16 v143, v116, v66 op_sel_hi:[0,1]\n\t" \
  "ds_write_addtid_b32 v138 offset:1728\n\t" \
  "ds_write_addtid_b32 v139 offset:2016\n\t" \
  "v_pk_fma_f16 v143, v116, v66, v143 op_sel:[1,1,0] op_sel_hi:[1,0,1] neg_lo:[0,1,0]\n\t" \
  "v_pk_mul_f16 v144, v60, v85 op_sel_hi:[0,1]\n\t" \
  "ds_write_addtid_b32 v70 offset:2304\n\t" \
  "v_pk_mul_f16 v145, v63, v85 op_sel_hi:[0,1]\n\t" \
  "v_pk_fma_f16 v144, v60, v85, v144 op_sel:[1,1,0] op_sel_hi:[1,0,1] neg_lo:[0,1,0]\n\t" \
  "ds_write_addtid_b32 v140 offset:2592\n\t" \
  "v_pk_fma_f16 v145, v63, v85, v145 op_sel:[1,1,0] op_sel_hi:[1,0,1] neg_lo:[0,1,0]\n\t" \
  "v_pk_mul_f16 v146, v89, v85 op_sel_hi:[0,1]\n\t" \
  "ds_write_addtid_b32 v114 offset:2880\n\t" \
  "v_pk_mul_f16 v147, v70, v85 op_sel_hi:[0,1]\n\t" \
  "v_pk_fma_f16 v146, v89, v85, v146 op_sel:[1,1,0] op_sel_hi:[1,0,1] neg_lo:[0,1,0]\n\t" \
  "ds_write_addtid_b32 v141 offset:3168\n\t" \
  "v_pk_fma_f16 v147, v70, v85, v147 op_sel:[1,1,0] op_sel_hi:[1,0,1] neg_lo:[0,1,0]\n\t" \
  "v_pk_mul_f16 v148, v114, v85 op_sel_hi:[0,1]\n\t" \
  "ds_write_addtid_b32 v115 offset:3456\n\t" \
  "v_pk_mul_f16 v149, v115, v85 op_sel_hi:[0,1]\n\t" \
  "v_pk_fma_f16 v148, v114, v85, v148 op_sel:[1,1,0] op_sel_hi:[1,0,1] neg_lo:[0,1,0]\n\t" \
  "ds_write_addtid_b32 v142 offset:3744\n\t" \
  "v_pk_fma_f16 v149, v115, v85, v149 op_sel:[1,1,0] op_sel_hi:[1,0,1] neg_lo:[0,1,0]\n\t" \
  "v_pk_mul_f16 v150, v116, v85 op_sel_hi:[0,1]\n\t" \
  "ds_write_addtid_b32 v116 offset:4032\n\t" \
  "ds_write_addtid_b32 v143 offset:4320\n\t" \
  "v_pk_fma_f16 v150, v116, v85, v150 op_sel:[1,1,0] op_sel_hi:[1,0,1] neg_lo:[0,1,0]\n\t" \
  "ds_read_b128 v[152:155], %[sr] offset:0\n\t" \
  "ds_read_b128 v[156:159], %[sr] offset:64\n\t" \
  "ds_read_b128 v[160:163], %[sr] offset:128\n\t" \
  "ds_read_b128 v[164:167], %[sr] offset:192\n\t" \
  "v_mul_f32 v180, %[sx], %[dt]\n\t" \
  "s_waitcnt lgkmcnt(14)\n\t" \
  "ds_write_addtid_b32 v85 offset:0\n\t" \
  "v_mfma_f32_16x16x32_f16 v[168:171], v[122:125], v[90:93], 0\n\t" \
  "ds_write_addtid_b32 v144 offset:288\n\t" \
  "ds_write_addtid_b32 v145 offset:576\n\t" \
  "v_mfma_f32_16x16x32_f16 v[168:171], v[126:129], v[94:97], v[168:171]\n\t" \
  "ds_write_addtid_b32 v146 offset:864\n\t" \
  "ds_write_addtid_b32 v147 offset:1152\n\t" \
  "v_mfma_f32_16x16x32_f16 v[168:171], v[130:133], v[98:101], v[168:171]\n\t" \
  "ds_write_addtid_b32 v148 offset:1440\n\t" \
  "ds_write_addtid_b32 v149 offset:1728\n\t" \
  "v_mfma_f32_16x16x32_f16 v[168:171], v[134:137], v[102:105], v[168:171]\n\t" \
  "ds_write_addtid_b32 v150 offset:2016\n\t" \
  "ds_write_addtid_b32 v106 offset:2304\n\t" \
  "ds_read_b128 v[182:185], %[sr] offset:0\n\t" \
  "ds_read_b128 v[186:189], %[sr] offset:64\n\t" \
  "ds_read_b128 v[190:193], %[sr] offset:128\n\t" \
  "ds_read_b128 v[194:197], %[sr] offset:192\n\t" \
  "s_waitcnt lgkmcnt(13)\n\t" \
  "v_mfma_f32_16x16x32_f16 v[172:175], v[152:155], v[90:93], 0\n\t" \
  "v_mfma_f32_16x16x32_f16 v[172:175], v[156:159], v[94:97], v[172:175]\n\t" \
  "v_mfma_f32_16x16x32_f16 v[172:175], v[160:163], v[98:101], v[172:175]\n\t" \
  "v_mfma_f32_16x16x32_f16 v[172:175], v[164:167], v[102:105], v[172:175]\n\t" \
  "v_fmac_f32 v16, v180, v168\n\t" \
  "v_fmac_f32 v17, v180, v169\n\t" \
  "v_fmac_f32 v18, v180, v170\n\t" \
  "v_fmac_f32 v19, v180, v171\n\t" \
  "s_waitcnt lgkmcnt(3)\n\t" \
  "v_mfma_f32_16x16x32_f16 v[176:179], v[90:93], v[182:185], 0\n\t" \
  "s_waitcnt lgkmcnt(2)\n\t" \
  "v_mfma_f32_16x16x32_f16 v[176:179], v[94:97], v[186:189], v[176:179]\n\t" \
  "s_waitcnt lgkmcnt(1)\n\t" \
  "v_mfma_f32_16x16x32_f16 v[176:179], v[98:101], v[190:193], v[176:179]\n\t" \
  "s_waitcnt lgkmcnt(0)\n\t" \
  "v_mfma_f32_16x16x32_f16 v[176:179], v[102:105], v[194:197], v[176:179]\n\t" \
  "v_fmac_f32 v20, v180, v172\n\t" \
  "v_fmac_f32 v21, v180, v173\n\t" \
  "v_fmac_f32 v22, v180, v174\n\t" \
  "v_fmac_f32 v23, v180, v175\n\t" \
  "s_nop 0\n\t" \
  "s_nop 0\n\t" \
  "s_nop 0\n\t" \
  "s_nop 0\n\t" \
  "v_fmac_f32 v24, v180, v176\n\t" \
  "v_fmac_f32 v25, v180, v177\n\t" \
  "v_fmac_f32 v26, v180, v178\n\t" \
  "v_fmac_f32 v27, v180, v179\n\t"
#define GEN_CLOB "v32", "v33", "v34", "v35", "v36", "v37", "v38", "v39", "v40", "v41", "v42", "v43", "v44", "v45", "v46", "v47", "v48", "v49", "v50", "v51", "v52", "v53", "v54", "v55", "v56", "v57", "v58", "v59", "v60", "v61", "v62", "v63", "v64", "v65", "v66", "v67", "v68", "v69", "v70", "v71", "v72", "v73", "v74", "v75", "v76", "v77", "v78", "v79", "v80", "v81", "v82", "v83", "v84", "v85", "v86", "v87", "v88", "v89", "v90", "v91", "v92", "v93", "v94", "v95", "v96", "v97", "v98", "v99", "v100", "v101", "v102", "v103", "v104", "v105", "v106", "v107", "v108", "v109", "v110", "v111", "v112", "v113", "v114", "v115", "v116", "v117", "v118", "v119", "v120", "v121", "v122", "v123", "v124", "v125", "v126", "v127", "v128", "v129", "v130", "v131", "v132", "v133", "v134", "v135", "v136", "v137", "v138", "v139", "v140", "v141", "v142", "v143", "v144", "v145", "v146", "v147", "v148", "v149", "v150", "v151", "v152", "v153", "v154", "v155", "v156", "v157", "v158", "v159", "v160", "v161", "v162", "v163", "v164", "v165", "v166", "v167", "v168", "v169", "v170", "v171", "v172", "v173", "v174", "v175", "v176", "v177", "v178", "v179", "v180", "v181", "v182", "v183", "v184", "v185", "v186", "v187", "v188", "v189", "v190", "v191", "v192", "v193", "v194", "v195", "v196", "v197"

DEV void gen_front(const Params& p, int w, int wv, int lane) {
  f32x16 kacc = {};
  const int d0 = 10 * w, chB = ((wv & 2) ? 6 : 8) + (wv & 1), cB0 = wv >= 4 ? 2 : 0, tEnd = wv >= 4 ? 4 : wv >= 2 ? 6 : 5;
  u32 l0, l1, k0, k1;
  asm volatile("s_load_dword %0, %4, %6\n\ts_load_dword %1, %4, %7\n\ts_load_dword %2, %5, %6\n\ts_load_dword %3, %5, %7"
               : "=&s"(l0), "=&s"(l1), "=&s"(k0), "=&s"(k1) : "s"(p.ldt), "s"(p.dskip), "s"(((d0 + wv) >> 1) * 4), "s"(((d0 + chB) >> 1) * 4) : "memory");
  const u32 prm = S_PRM + 256 * wv + 768 * (wv >= 4 ? 6 + 4 * wv : wv >= 2 ? 6 * wv - 2 : 5 * wv);
  const int ar = min(lane >> 3, 5);
  const u16* src = ar == 1 ? p.Aim : p.Are; asm("" : "+v"(src));
  src = ar == 2 ? p.Bre : src; asm("" : "+v"(src));
  src = ar == 3 ? p.Bim : src; asm("" : "+v"(src));
  src = ar == 4 ? p.Cre : src; asm("" : "+v"(src));
  src = ar == 5 ? p.Cim : src;
  src += d0 * 192 + (lane & 7) * 8;
#pragma unroll
  for (int t = 0; t < 6; ++t)
    if (t < tEnd)
      asm volatile("s_mov_b32 m0, %1\n\ts_nop 0\n\tglobal_load_lds_dwordx4 %0, off\n\ts_nop 1" :: "v"(src + (t < 3 ? wv * 192 + t * 64 : chB * 192 + (cB0 + t - 3) * 64)), "s"(prm + t * 768) : "memory");
  KcAdr KA = kc_adr(lane);
  kc_zero(S_KC + wv * KC_ST, lane); kc_zero(S_KC + chB * KC_ST, lane);
  asm volatile("s_waitcnt lgkmcnt(0)" : "+s"(l0), "+s"(l1), "+s"(k0), "+s"(k1) :: "memory");
  asm volatile("" : "+v"(KA.t0), "+v"(KA.c1a), "+v"(KA.c1b), "+v"(KA.s1x), "+v"(KA.s1a), "+v"(KA.s1b), "+v"(KA.t2x), "+v"(KA.t2));
  const bool odd = wv & 1;
  const float dkA = odd ? bfhi(k0) : bflo(k0), dkB = odd ? bfhi(k1) : bflo(k1);
  float dt0 = bflo(pk(expf(odd ? bfhi(l0) : bflo(l0)), 0.f)), dt1 = bflo(pk(expf(odd ? bfhi(l1) : bflo(l1)), 0.f));
  asm volatile("" : "+v"(dt0), "+v"(dt1));
  const u32 scr = wv * SCR_ST, sr = scr + (lane & 15) * SCR_ROW + (lane >> 4) * 16;
  if (wv >= 4) waitv<2>(); else if (wv >= 2) waitv<4>(); else waitv<3>();
#pragma nounroll
  for (int t = 0; t < tEnd; ++t) {
    const float dt = t < 3 ? dt0 : dt1;
    u32 sx, sy;
    asm volatile("s_mov_b32 m0, %[m]\n\t" GEN_ASM : "+{v[16:31]}"(kacc), [sx] "=&s"(sx), [sy] "=&s"(sy)
                 : [pa] "v"(prm + t * 768 + lane * 2), [sr] "v"(sr), [dl] "v"(dt * 1.4426950408889634f), [dr] "v"(dt * 0.15915494309189535f), [dt] "v"(dt), [m] "s"(scr) : GEN_CLOB, "scc", "memory");
    if (t == 2 || t == 5) { kc_write(S_KC + (t == 2 ? wv : chB) * KC_ST, KA, kc_pack(kacc, t == 2 ? dkA : dkB, lane)); kacc = f32x16{}; }
    if (t == 0) waitv<0>();
  }
  if (!(wv & 2)) {
    if (wv < 2 && lane == 0) kacc[0] += dkB;
#pragma unroll
    for (int k = 0; k < 3; ++k) sts128(scr + k * 1024 + lane * 16, asu4(f32x4{kacc[4 * k], kacc[4 * k + 1], kacc[4 * k + 2], kacc[4 * k + 3]}));
  }
}

DEV void ssm_role(const Params& p, int w, int pm, int dn, __amdgpu_buffer_rsrc_t wsr, u64& epoch) {
  const int tid = threadIdx.x, lane = tid & 63, wv = __builtin_amdgcn_readfirstlane(tid >> 6);
  const int d0 = 10 * w, g4 = lane >> 4, q4 = (lane & 15) >> 2, p4 = lane & 3;
  if (wv >= 6) {
    u32x2 epv;
    ld_epoch(epv, p.ws);
    const int hw = wv - 6;
    const u32 im = S_UR + hw * 21504;
#pragma unroll
    for (int j = 0; j < 21; ++j) {
      const int un = min(j * 64 + lane, 1311), row = un / 41;
      glds1((const char*)p.u + w * 40960 + hw * 640, row * 1280 + min(un - row * 41, 39) * 16, im + j * 1024);
    }
    glds1((const char*)p.gw + hw * 640, min(lane, 39) * 16, S_GW + hw * 1024);
#pragma unroll
    for (int k = 0; k < 7; ++k) { const int q = min(hw * 7 + k, 12); glds1((const char*)&conv_tab + q * 1024, lane * 16, q < 9 ? S_CT + q * 1024 : S_GW + 3584 + (q - 9) * 1024); }
    { const int wc = min(w, 47);
#pragma unroll
      for (int j = 0; j < 10; ++j) { const int un = j * 64 + lane, row = un / 20;
        glds1((const char*)p.wout + (wc >> 1) * 32 * 1280 + (wc & 1) * 640 + hw * 320, row * 1280 + (un - row * 20) * 16, (hw ? S_WO1 : S_WO0) + j * 1024); } }
    if (lane < 8) sts32(S_GW + 3072 + lane * 4, 0);
    __builtin_amdgcn_s_setprio(2);
    waitv<17>();
    const u64 tag = mk_tag(rd_epoch(epv));
    if (lane == 0) sts64(S_GW + 3104, epv);
    const u32 ra = im + (27 - 8 * p4 - q4) * 656 + g4 * 8;
#pragma unroll
    for (int h = 0; h < 2; ++h) {
      u32x4 tv[10];
#pragma unroll
      for (int j = 0; j < 10; ++j) tv[j] = ldstr2(ra + 4 * 656 + (10 * h + j) * 32, ra + (10 * h + j) * 32);
#pragma unroll
      for (int j = 0; j < 10; ++j) st16(tv[j], wsr, (4 * g4 + p4) * 64 + q4 * 16, WS_UT + w * 40960 + (hw * 20 + 10 * h + j) * 1024);
    }
    f32x4 g0 = {}, g1 = {};
#pragma unroll
    for (int j = 0; j < 10; ++j) { const u32x4 gv = lds128(S_GW + hw * 1024 + j * 64 + g4 * 16);
      g0 = mfma16(gv, lds128(im + (lane & 15) * 656 + j * 64 + g4 * 16), g0); g1 = mfma16(gv, lds128(im + (16 + (lane & 15)) * 656 + j * 64 + g4 * 16), g1); }
    if (lane < 32) sts32(S_GW + 2560 + hw * 128 + lane * 4, asu(lane < 16 ? g0[0] : g1[0]));
    asm volatile("s_waitcnt vmcnt(0) lgkmcnt(0)" ::: "memory");
    st_flag(p.ws + WS_FU + (lane * 128 + 2 * w + hw) * 8, tag);
#pragma unroll
    for (int k = 0; k < 8; ++k)
      touch(p.wsp, (32 * w + 16 * hw + 8 * (k & 1) + (lane >> 3)) * 4096 + (k >> 1) * 1024 + (lane & 7) * 128, S_GW + 2048);
#pragma nounroll
    for (u32 sp = 0; sp < SPIN_CAP; ++sp) {
      __builtin_amdgcn_s_sleep(4);
      const u64 f0_ = ld_flag(p.ws + WS_FU + w * 1024 + lane * 8), f1_ = ld_flag(p.ws + WS_FU + w * 1024 + 512 + lane * 8);
      if (__all(f0_ == tag && f1_ == tag)) break;
    }
#pragma unroll
    for (int k = 0; k < 20; ++k)
      glds1s(p.ws + WS_UT + (48 - 16 * (k & 3)) * 40960 + (d0 + 5 * hw + k / 4) * 64, (15 - (lane >> 2)) * 40960 + (lane & 3) * 16, S_UR + (5 * hw + k / 4) * UR_ST + (k & 3) * 1024);
#pragma unroll
    for (int k = 0; k < 5; ++k) { if (lane < 32) sts128(S_UR + (5 * hw + k) * UR_ST + 4096 + lane * 16, u32x4{0, 0, 0, 0}); }
    { const int row = 16 * hw + (lane & 15);
      const float sg = asf(*LP(const u32, S_GW + 2560 + row * 4)) + asf(*LP(const u32, S_GW + 2688 + row * 4));
      __builtin_amdgcn_raw_buffer_store_b32(asu(__builtin_amdgcn_rcpf(1.0f + __expf(-sg))), wsr, lane < 16 ? WS_G + (32 * w + row) * 4 : WS_END, 0, 16); }
    { const u32 rw = (hw ? S_WO1 : S_WO0) + (8 * g4 + q4) * 320 + p4 * 8;
      u32x4 tv[10];
#pragma unroll
      for (int j = 0; j < 10; ++j) tv[j] = ldstr2(rw + j * 32, rw + 4 * 320 + j * 32);
      if (w < 48) {
#pragma unroll
        for (int j = 0; j < 10; ++j) st16(tv[j], wsr, lane * 16, WS_WP + ((((w & 1) * 4 + hw * 2 + j / 5) * 24 + (w >> 1)) * 5 + j % 5) * 1024);
      } }
    __builtin_amdgcn_s_setprio(0);
    if (w < 48) waitv<11>(); else waitv<1>();
  }
  bar();
  { const u32x2 e = lds64(S_GW + 3104); epoch = ((u64)(u32)__builtin_amdgcn_readfirstlane(e[1]) << 32) | (u32)__builtin_amdgcn_readfirstlane(e[0]); }
  const u64 tag = mk_tag(epoch);
  u32x4 ct[13];
#pragma unroll
  for (int k = 0; k < 13; ++k) ct[k] = lds128((k < 9 ? S_CT + k * 1024 : S_GW + 3584 + (k - 9) * 1024) + lane * 16);
  if (wv < 2) {
    f32x16 kacc;
#pragma unroll
    for (int k = 0; k < 3; ++k) {
      const f32x4 x = asf4(lds128(wv * SCR_ST + k * 1024 + lane * 16)) + asf4(lds128((wv + 4) * SCR_ST + k * 1024 + lane * 16));
#pragma unroll
      for (int j = 0; j < 4; ++j) kacc[4 * k + j] = x[j];
    }
    kc_write(S_KC + (8 + wv) * KC_ST, kc_adr(lane), kc_pack(kacc, 0.f, lane));
  }
  bar();
  if (wv >= 6) { waitv<0>(); if (lane == 0) st_flag(p.ws + WS_FW + (2 * w + wv - 6) * 8, tag); }
  const int hf = wv >> 2, jj = hf ? 7 - wv : wv, chb = 5 * hf;
  auto CT = [&](int i) { return ct[i >> 2][i & 3]; };
  u32x32 CA; u32x16 CB;
#pragma unroll
  for (int k = 0; k < 32; ++k) CA[k] = CT(k);
#pragma unroll
  for (int k = 0; k < 16; ++k) CB[k] = CT(32 + k);
  f32x32 acc;
  auto segment = [&](int ch, int first, int n) {
    const u32 kb = S_KC + ch * KC_ST;
    u32x16 AD;
    AD[0] = S_UR + ch * UR_ST + CB[8];
#pragma unroll
    for (int k = 1; k < 11; ++k) AD[k] = kb + (k < 8 ? CB[8 + k] : CT(40 + k));
    asm volatile(CONV_ASM : "+{v[16:47]}"(acc), [n] "+s"(n) : "{v[48:79]}"(CA), "{v[80:95]}"(CB), "{v[96:111]}"(AD), [p] "s"(first) : CONV_CLOB, "scc", "memory");
  };
  auto store_out = [&](int ch) {
    const u32 ln = lane_now();
    const u32 vo = ((ln >> 4) & 1) * 512 + (ln & 15) * 32 + (ln >> 5) * 16;
#pragma unroll
    for (int R = 0; R < 8; R += 2) {
      auto r0 = __builtin_amdgcn_permlane16_swap(pk(acc[4 * R], acc[4 * R + 1]), pk(acc[4 * R + 4], acc[4 * R + 5]), false, false);
      auto r1 = __builtin_amdgcn_permlane16_swap(pk(acc[4 * R + 2], acc[4 * R + 3]), pk(acc[4 * R + 6], acc[4 * R + 7]), false, false);
      st16(u32x4{r0[0], r1[0], r0[1], r1[1]}, wsr, vo, WS_A + (d0 + ch) * 4096 + R * 512);
    }
  };
  auto part_adr = [&](int slot) { u32 pa = slot * 8192 + lane_now() * 16; asm volatile("" : "+v"(pa)); return pa; };
  { float z0 = 0.f; asm volatile("" : "+v"(z0));
#pragma unroll
    for (int k = 0; k < 32; ++k) acc[k] = z0; }
#pragma nounroll
  for (int sgm = 0; sgm < 2; ++sgm) {
    if (hf != sgm) __builtin_amdgcn_s_setprio(1); else __builtin_amdgcn_s_setprio(0);
    segment(chb + jj + sgm, sgm ? 0 : jj, sgm ? 1 + jj : 4 - jj);
    if (sgm == 1 || jj == 0) store_out(chb + jj + sgm);
    if (sgm == 0) {
      if (jj != 0) {
        const u32 pa = part_adr(hf * 3 + jj - 1);
#pragma unroll
        for (int R = 0; R < 8; ++R) sts128(pa + R * 1024, asu4(f32x4{acc[4 * R], acc[4 * R + 1], acc[4 * R + 2], acc[4 * R + 3]}));
        asm volatile("s_waitcnt lgkmcnt(0)" ::: "memory");
        __hip_atomic_store(LP(u32, S_GW + 3072 + (hf * 3 + jj - 1) * 4), pa | 1u, __ATOMIC_RELAXED, __HIP_MEMORY_SCOPE_WORKGROUP);
      }
      if (jj == 1) {
        const u32 ln = lane_now();
        int dd = dn; asm volatile("" : "+s"(dd));
#pragma nounroll
        for (u32 sp = 0; sp < SPIN_CAP; ++sp) {
          const bool f0_ = flag_ok(wsr, WS_FW + ln * 8, tag), f2_ = flag_ok(wsr, WS_FW + 512 + ln * 8, tag), f1_ = flag_ok(wsr, WS_FZ + ((pm * 8 + dn) * 64 + 32 + min(ln, 5u)) * 8, tag);
          if (__all(f0_ && f1_ && f2_)) break;
          asm volatile("" ::: "memory");
          __builtin_amdgcn_s_sleep(2);
        }
#pragma unroll
        for (int k = 0; k < 15; ++k) glds1s(p.ws + WS_WP + dd * 122880 + hf * 15360 + k * 1024, ln * 16, RB + (1 + hf) * 16384 + k * 1024);
      }
      if (jj < 3) {
#pragma nounroll
        for (u32 sp = 0; sp < SPIN_CAP && __builtin_amdgcn_readfirstlane(__hip_atomic_load(LP(u32, S_GW + 3072 + (hf * 3 + jj) * 4), __ATOMIC_RELAXED, __HIP_MEMORY_SCOPE_WORKGROUP)) == 0; ++sp) __builtin_amdgcn_s_sleep(1);
        asm volatile("" ::: "memory");
        const u32 pa = part_adr(hf * 3 + jj);
#pragma unroll
        for (int R = 0; R < 8; ++R) {
          const f32x4 t = asf4(lds128(pa + R * 1024));
#pragma unroll
          for (int k = 0; k < 4; ++k) acc[4 * R + k] = t[k];
        }
      } else {
        float z0 = 0.f; asm volatile("" : "+v"(z0));
#pragma unroll
        for (int k = 0; k < 32; ++k) acc[k] = z0;
      }
    }
  }
  __builtin_amdgcn_s_setprio(0);
  bar();
}

constexpr int H_PAD = 12;
constexpr int RETRY_SLEEP = 8;
constexpr int DZ = 2;
constexpr int need_of(int s) { return s < 10 ? 3 * (s / 2) + 1 + (s & 1) : s + 5; }
constexpr int cmin(int a, int b) { return a < b ? a : b; }
constexpr int STEP_SA = 13;
constexpr int STEP_SB = 14;
constexpr int STEP_SF = 17;
constexpr int SF_PCS = 2;
constexpr int p1_in(int s) { constexpr int t[20] = {4, 4, 4, 2, 4, 2, 2, 2, 2, 2, 2, 4, 2, 2, 2, 2, 2, 2, 2, 2}; return s >= STEP_SF ? SF_PCS : s > STEP_SB ? 0 : t[s]; }
constexpr int p1_before(int s) { int n = 10; for (int i = 0; i < s; ++i) n += p1_in(i) + (i == 9 ? 4 : 0); return n; }
static_assert(p1_before(20) <= 64 && p1_before(19) <= 62 && p1_before(18) <= 60 && p1_before(17) <= 58, "ring slots");
static_assert(p1_before(STEP_SB + 1) >= 50, "all w_in chunks are requested before the stores");

constexpr int pcs_before(int i) {
  int n = p1_before(20);
  for (int s = 0; s < i; ++s) {
    int want = s < 12 ? 2 : s < 14 ? 3 : s < 16 ? 6 : 0;
    int lim = s < 11 ? cmin(82, 66 + 2 * s) : 68 + 2 * cmin(s, 15);
    n = cmin(n + want, lim);
  }
  return n;
}
constexpr int zl_in(int s) { return s + DZ < 16 ? 4 : 0; }
constexpr int g2_wait(int i) {
  int nz = 0; for (int s = i - DZ; s < i; ++s) nz += (s < 0 ? 4 : zl_in(s) + pcs_before(s + 1) - pcs_before(s));
  if (i < 16 && 52 + 2 * i > pcs_before(0)) {
    int sg = 0; while (pcs_before(sg + 1) < 52 + 2 * i) ++sg;
    int ns = 0; for (int s = sg + 1; s < i; ++s) ns += zl_in(s) + pcs_before(s + 1) - pcs_before(s);
    nz = cmin(nz, ns);
  }
  return nz;
}
constexpr bool g2_ok() { for (int i = 1; i < 16; ++i) if (pcs_before(i - 1 > 0 ? i - 1 : 0) < 52 + 2 * i) return false; return true; }
static_assert(g2_ok() && pcs_before(11) == 82 && pcs_before(16) == 98 && DZ <= 2, "every w_sp chunk is requested at least two steps ahead");

#define CODE_WARM if (wv == 7) touch(pc_, min((u32)((idx - 24) * 40 + min(lane, 39)) * 128u, rem_ - 4), S_GW + 2048);
#define WIN_WARM if (wv == 7) { for (int k = 0; k < 2; ++k) { const int l_ = (xcd * 8 + idx - 24) * 120 + min(k * 64 + lane, 119); touch(p.win, (l_ / 12) * 3072 + (l_ % 12) * 128, S_GW + 2048); } }
constexpr int SSM_FIRST = 0;
__global__ __launch_bounds__(512, 2) void fused(Params p) {
  const int tid = threadIdx.x, lane = tid & 63, wv = __builtin_amdgcn_readfirstlane(tid >> 6);
  const int xcd = blockIdx.x & 7, idx = blockIdx.x >> 3, xm = xcd & 3, xn = xcd >> 2;
  asm volatile("" :: "s"(p.u), "s"(p.Are), "s"(p.Aim), "s"(p.Bre), "s"(p.Bim), "s"(p.Cre), "s"(p.Cim), "s"(p.ldt), "s"(p.dskip));
  const char* pc_; u32 rem_; asm volatile("s_getpc_b64 %0\nLpc%=:\n\ts_mov_b32 %1, Lcode_tail-Lpc%=" : "=&s"(pc_), "=s"(rem_));
  u64 epoch, tag;
  const __amdgpu_buffer_rsrc_t wsr = __builtin_amdgcn_make_buffer_rsrc(p.ws, 0, WS_END, 0x00020000);
  const int pm = xm * 8 + (idx & 7), dn = xn * 4 + (idx >> 3);
  const u32 dstw = RB + wv * 2048;
  const int l31 = lane & 31, hh = lane >> 5, l15 = lane & 15, g4 = lane >> 4, q4 = l15 >> 2, p4 = lane & 3;
  u32 vo;
  auto mkvo = [&]() { vo = lane_now() * 16; };
  const char* obase = p.ws + WS_WP + dn * 122880 + wv * 2048;
  auto opiece = [&](auto Q) { constexpr int c = Q / 2, j = Q & 1; glds1s(obase + c * 15360 + ((j && wv < 7) ? 1024 : 0), vo, (c == 7 ? -1 : c + 1) * 16384 + j * 1024 + dstw); };
  const int tn = xn * 3 + (idx >> 3);
  const int st = wv & 1, ft = wv >> 1;
  const u32 sl0 = 16 * (l15 ^ ((g4 << 2) | ((2 * wv) & 3))), sl1 = 16 * (l15 ^ ((g4 << 2) | ((2 * wv + 1) & 3)));
  const u32 row0 = 8 * wv + g4, row1 = row0 + 4;
  const u32 vx0 = row0 * 1280 + sl0, vx1 = row1 * 1280 + sl1, vw0 = row0 * 3072 + sl0, vw1 = row1 * 3072 + sl1, vs0 = row0 * 4096 + sl0, vs1 = row1 * 4096 + sl1;
  const char* xbase = (const char*)p.u + (size_t)pm * 64 * 1280;
  const char* wbase = (const char*)p.win + tn * 256;
  const char* sbase = (const char*)p.wsp + (size_t)pm * 64 * 4096;
  const int rot = (pm + 2 * tn) % 5;
  auto kg = [&](int g) { const int t = g + rot; return t >= 5 ? t - 5 : t; };
  auto piece = [&](auto Q) {
    constexpr int c = Q / 2, j = Q & 1;
    const u32 dst = (c & 7) * 16384 + j * 1024 + dstw;
    if constexpr (c >= 41) opiece(IC<(Q - 82 + 14) % 16>{});
    else if constexpr (c >= 25) glds1(sbase + (c - 25) * 256, j ? vs1 : vs0, dst);
    else if constexpr (c >= 15) glds1(wbase + (2 * kg((c - 15) / 2) + ((c - 15) & 1)) * 64 * 3072, j ? vw1 : vw0, dst);
    else if constexpr (c % 3 == 1) glds1(xbase + kg(c / 3) * 256, j ? vx1 : vx0, dst);
    else glds1(wbase + 1536 + (2 * kg(c / 3) + (c % 3) / 2) * 64 * 3072, j ? vw1 : vw0, dst);
  };
  if (idx >= 24) { CODE_WARM; WIN_WARM; }
  else sfor<0, 5>([&](auto C) { piece(IC<2 * C>{}); piece(IC<2 * C + 1>{}); if constexpr (C >= 1 && C < 4) bar0(); });
  { int r2 = idx; asm volatile("" : "+s"(r2)); if (__builtin_expect(r2 >= 24 && wv < 6, 1)) gen_front(p, xcd * 8 + idx - 24, wv, lane); }
  bool early = false;
  int role = idx; asm volatile("" : "+s"(role));
  if (__builtin_expect(role >= 24, SSM_FIRST)) {
    const int w = xcd * 8 + (idx - 24);
    ssm_role(p, w, pm, dn, wsr, epoch);
    tag = mk_tag(epoch);
    mkvo();
    sfor<4, 10>(opiece);
    waitv<6>();
    bar();
    if (wv == 0 && vo == 0) st_flag(p.ws + WS_FA + w * 8, tag);
    sfor<10, 16>(opiece);
    if (wv == 0) {
      const char* hw_ = p.ws + WS_H + ((4 * pm * 24 + 4 * min(vo >> 4, 5u)) * 64) * 16;
      early = !__all(__hip_atomic_load((GAS u32*)hw_, RLX_AGENT) != POISON);
    }
    waitv<0>();
    bar();
  } else {
    u32x2 epv;
    const u32 swr = ((l31 & 3) << 2) | ((l31 >> 2) & 3);
    const int blk = g4 & 1;
    u32 tro[2];
#pragma unroll
    for (int t = 0; t < 2; ++t) tro[t] = (8 * hh + 4 * t + q4) * 256 + 16 * ((4 * ft + 2 * blk + (p4 >> 1)) ^ ((q4 << 2) | ((2 * hh + t) & 3))) + 8 * (p4 & 1) + RB;
    auto hdst = [&]() { int ln = lane; asm volatile("" : "+v"(ln)); return WS_H + ((((4 * pm + 2 * st + ((ln >> 4) & 1)) * 24 + 4 * tn + ft) * 4 + (ln >> 5)) * 16 + (ln & 15)) * 16; };
    u32 bspr; u32x4 lw;
    const u32 fea = WS_FZ + ((pm * 8 + dn) * 64 + min(lane, 37)) * 8;
    f32x16 zacc = {};
    {
      u32x4 xf[40], wf[2][4];
      const u32 xrow = RB + (st * 32 + l31) * 256;
      auto mm = [&](auto S) { constexpr int s = S, j = s % 10;
#pragma unroll
        for (int ks = 0; ks < 4; ++ks) zacc = mfma32(wf[s & 1][ks], xf[4 * j + ks], zacc); };
      u32x4 sg[3], zo[2]; u32 zp[8], tw, tr_;
      const u32 sgoff = WS_STAT + ((pm * 6 + 3 * hh) * 64 + st * 32 + l31) * 16;
      auto step = [&](auto S) {
        constexpr int s = S, j = s % 10, q0 = p1_before(s), nq = p1_in(s);
        waitv<s == STEP_SF ? 0 : p1_before(s) - 2 * need_of(s) - 2 + (s > STEP_SB && s < STEP_SF ? 2 : 0) + (s >= 10 && need_of(s) <= 20 ? 2 : 0)>();
        bar();
        if constexpr (s == STEP_SF) { if (tid < 40) st_flag(p.ws + WS_FZ + (hh ? (pm * 8 + l31) * 64 + 32 + tn : (l31 * 8 + dn) * 64 + pm) * 8, tag); SB(); }
        if constexpr (s == 10) {
          if (wv == 0) {
            float a = 0.f, b = 0.f;
#pragma unroll
            for (int k = 0; k < 4; ++k) { u32x2 v = lds64(G_SCR + 16384 + (k * 64 + lane) * 8); a += asf(v[0]); b += asf(v[1]); }
            st16(u32x4{asu(a), (u32)tag, asu(b), (u32)tag}, wsr, WS_STAT + ((pm * 6 + tn) * 64 + lane) * 16, 0);
          }
          SB();
        }
        if constexpr (s == 0) ld_epoch(epv, p.ws);
        if constexpr (s == STEP_SA) { ldz<0>(sg[0], sgoff, wsr, 0); ldz<1024>(sg[1], sgoff, wsr, 0); ldz<2048>(sg[2], sgoff, wsr, 0); }
        constexpr u32 wsl = ((s < 10 ? 3 * (s / 2) + 2 * (s & 1) : s + 5) & 7) * 16384;
        sfor<0, 4>([&](auto KS) {
          constexpr int ks = KS;
          if constexpr (nq == 4) piece(IC<q0 + ks>{});
          if constexpr (nq == 2 && (ks & 1) == 0) piece(IC<q0 + ks / 2>{});
          if constexpr (ks < 2) {
#pragma unroll
            for (int k2 = 2 * ks; k2 < 2 * ks + 2; ++k2) {
              wf[s & 1][k2] = ldstr2(wsl + k2 * 4096 + tro[0], wsl + k2 * 4096 + tro[1]);
              if constexpr (s < 10) xf[4 * j + k2] = lds128(((3 * (s / 2) + 1) & 7) * 16384 + xrow + 16 * ((8 * (s & 1) + 2 * k2 + hh) ^ swr));
            }
          }
          SB();
          if constexpr (j > 0) zacc = mfma32(wf[(s - 1) & 1][ks], xf[4 * (j - 1) + ks], zacc);
          SB();
          if constexpr (s == STEP_SB && ks == 1) {
            asm volatile("s_waitcnt vmcnt(%3)" : "+v"(sg[0]), "+v"(sg[1]), "+v"(sg[2]) : "n"(p1_before(STEP_SB) + 1 - p1_before(STEP_SA)) : "memory");
#pragma nounroll
            for (u32 sp = 0; sp < SPIN_CAP; ++sp) {
              const u32 t32 = (u32)tag;
              if (__all(sg[0][1] == t32 && sg[0][3] == t32 && sg[1][1] == t32 && sg[1][3] == t32 && sg[2][1] == t32 && sg[2][3] == t32)) break;
              ldz<0>(sg[0], sgoff, wsr, 0); ldz<1024>(sg[1], sgoff, wsr, 0); ldz<2048>(sg[2], sgoff, wsr, 0);
              asm volatile("s_waitcnt vmcnt(0)" : "+v"(sg[0]), "+v"(sg[1]), "+v"(sg[2]) :: "memory");
            }
            const float a = asf(sg[0][0]) + asf(sg[1][0]) + asf(sg[2][0]), b = asf(sg[0][2]) + asf(sg[1][2]) + asf(sg[2][2]);
            auto sa = __builtin_amdgcn_permlane32_swap(asu(a), asu(a), false, false), sb = __builtin_amdgcn_permlane32_swap(asu(b), asu(b), false, false);
            const float m = (asf(sa[0]) + asf(sa[1])) * (1.0f / 768.0f), var = fmaxf((asf(sb[0]) + asf(sb[1])) * (1.0f / 768.0f) - m * m, 0.f), rs = __builtin_amdgcn_rsqf(var + 1e-5f), mr = -m * rs;
#pragma unroll
            for (int k = 0; k < 4; ++k) sts64(tw ^ (16 * k), u32x2{pk2(bf2(zp[2 * k]) * rs + mr), pk2(bf2(zp[2 * k + 1]) * rs + mr)});
            zo[0] = ldstr2(tr_, tr_ ^ 272); zo[1] = ldstr2(tr_ + 1024, (tr_ ^ 272) + 1024);
            SB();
          }
          if constexpr (s == STEP_SB && ks == 3) {
            st16(zo[0], wsr, lane * 16, WS_Z2N + ((4 * tn + ft) * 128 + 4 * pm + 2 * st) * 1024); st16(zo[1], wsr, lane * 16, WS_Z2N + ((4 * tn + ft) * 128 + 4 * pm + 2 * st + 1) * 1024);
            SB();
          }
        });
        if constexpr (s == STEP_SA) {
          const int row = 8 * hh + q4;
          tw = G_SCR + wv * 2048 + l31 * 64 + 8 * (hh ^ ((l31 >> 1) & 7)); tr_ = G_SCR + wv * 2048 + row * 64 + 8 * ((4 * blk + p4) ^ ((row >> 1) & 7));
          asm volatile("" : "+v"(tw), "+v"(tr_));
        }
      };
      sfor<0, 10>(step);
      sfor<38, 42>(piece);
      { const u32x4 pz = {POISON, POISON, POISON, POISON}; const u32 hd = hdst();
        st16(pz, wsr, hd, 0); st16(pz, wsr, hd + 512, 0); }
      mm(IC<9>{});
      epoch = get_epoch(epv); tag = mk_tag(epoch);
      { f32x2 s1 = {0.f, 0.f}, s2 = {0.f, 0.f};
        sfor<0, 8>([&](auto R) { constexpr int r = R;
          f32x2 z = gelu2(f32x2{zacc[2 * r], zacc[2 * r + 1]}); zp[r] = pk2(z); s1 += z; s2 += z * z; });
        auto sw = __builtin_amdgcn_permlane32_swap(asu(s1[0] + s1[1]), asu(s2[0] + s2[1]), false, false);
        sts32(G_SCR + 16384 + (ft * 64 + st * 32 + l31) * 8 + hh * 4, asu(asf(sw[0]) + asf(sw[1])));
        zacc = f32x16{};
        step(IC<10>{});
        asm volatile("global_load_ushort %0, %1, %2" : "=v"(bspr) : "v"((pm * 64 + st * 32 + l31) * 2), "s"(p.bsp) : "memory");
        SB();
      }
      sfor<11, 20>(step);
      mm(IC<19>{});
    }
    u32 z1p[8];
    mkvo();
    asm volatile("" : "+v"(bspr));
    if (wv == 0) poll(p.ws + fea, tag);
    bar();
    {
      const u32 zoff = WS_Z2N + (((4 * tn + ft) * 128 + 4 * st) * 64 + lane) * 16;
      u32x4 zf[DZ + 2][4], sf[2][8];
      sfor<0, DZ>([&](auto I) { sfor<0, 4>([&](auto K) { ldz<K * 1024>(zf[I][K], zoff, wsr, I * 8192); }); bar0(); });
      f32x16 acc[2];
      u32 so[2][2][4];
#pragma unroll
      for (int k = 0; k < 4; ++k) {
        so[0][0][k] = (st * 32 + l31) * 256 + 16 * ((8 * st + 2 * k + hh) ^ swr); so[0][1][k] = (so[0][0][k] ^ 8192) + RB; so[0][0][k] += RB; so[1][0][k] = so[0][0][k] + 65536; so[1][1][k] = so[0][1][k] + 65536;
        asm volatile("" : "+v"(so[0][0][k]), "+v"(so[0][1][k]), "+v"(so[1][0][k]), "+v"(so[1][1][k]));
      }
      sfor<0, 17>([&](auto I) {
        constexpr int i = I, q0 = pcs_before(i), nq = pcs_before(i + 1) - pcs_before(i);
        if constexpr (i >= 1) {
          waitz<g2_wait(i)>(zf[(i - 1) % (DZ + 2)]);
        }
        if constexpr (i >= 1 && i < 16) bar();
        if constexpr (i == 8) asm volatile("buffer_load_dwordx4 %0, %1, %2, 0 offen sc1" : "=v"(lw) : "v"(WS_FW + lane * 16), "s"(wsr) : "memory");
        if constexpr (i == 9 + DZ) {
          asm volatile("" : "+v"(lw));
          if (!__all(lw[0] == (u32)tag && lw[1] == (u32)(tag >> 32) && lw[2] == (u32)tag && lw[3] == (u32)(tag >> 32))) { poll(p.ws + WS_FW + lane * 8, tag); poll(p.ws + WS_FW + 512 + lane * 8, tag); }
          SB();
        }
        constexpr u32 ss = ((25 + i) & 3) * 16384, sh = ((25 + i) >> 2) & 1;
        sfor<0, 8>([&](auto M) {
          constexpr int m = M;
          if constexpr (zl_in(i) > 0) {
            if constexpr (m == 0 || m == 1 || m == 3 || m == 5) { constexpr int k = m == 0 ? 0 : (m + 1) / 2; ldz<k * 1024>(zf[(i + DZ) % (DZ + 2)][k], zoff, wsr, (i + DZ) * 8192); }
            if constexpr (m == 2 && nq > 0) piece(IC<q0>{});
            if constexpr (m == 4 && nq > 1) piece(IC<q0 + 1>{});
            if constexpr (m == 6 && nq > 2) piece(IC<q0 + 2>{});
            if constexpr (m == 7 && nq > 3) piece(IC<q0 + 3>{});
          } else if constexpr (m < nq) piece(IC<q0 + m>{});
          if constexpr (i < 16 && m < 2) {
#pragma unroll
            for (int k = 2 * m; k < 2 * m + 2; ++k) { sf[i & 1][2 * k] = lds128(ss + so[sh][0][k]); sf[i & 1][2 * k + 1] = lds128(ss + so[sh][1][k]); }
          }
          SB();
          if constexpr (i >= 1 && i < 9 && m == 6) { z1p[i - 1] = pk2(gelu2(f32x2{zacc[2 * i - 2], zacc[2 * i - 1]})); asm volatile("" : "+v"(z1p[i - 1])); }
          constexpr int mm_ = i < 16 ? m : (m < 4 ? 2 * m + 1 : 2 * (m - 4));
          if constexpr (i == 1 && m < 2) mfma32z(acc[m], zf[0][0], sf[0][m]);
          else if constexpr (i >= 1) mfma32v(acc[mm_ & 1], zf[(i - 1) % (DZ + 2)][mm_ / 2], sf[(i - 1) & 1][mm_]);
          SB();
        });
      });
      mfma_done(acc[1]);
#pragma unroll
      for (int k = 0; k < 2; ++k)
        sts128(G_SCR + wv * 2048 + k * 1024 + lane * 16, u32x4{pk(acc[1][8 * k], acc[1][8 * k + 1]), pk(acc[1][8 * k + 2], acc[1][8 * k + 3]), pk(acc[1][8 * k + 4], acc[1][8 * k + 5]), pk(acc[1][8 * k + 6], acc[1][8 * k + 7])});
      const float bsp = bflo(bspr);
      mfma_done(acc[0]);
      f32x16 G = acc[0] + bsp;
      const u32 hd = hdst();
      waitv<0>();
      bar();
      u32 hp[8];
#pragma unroll
      for (int k = 0; k < 2; ++k) {
        const u32x4 o = lds128(G_SCR + (wv ^ 1) * 2048 + k * 1024 + lane * 16);
#pragma unroll
        for (int j = 0; j < 4; ++j) hp[4 * k + j] = unp(pk2(bf2(z1p[4 * k + j]) * (f32x2{G[8 * k + 2 * j], G[8 * k + 2 * j + 1]} + bf2(o[j]))));
      }
#pragma unroll
      for (int k = 0; k < 2; ++k) {
        auto r0 = __builtin_amdgcn_permlane32_swap(hp[4 * k], hp[4 * k + 2], false, false);
        auto r1 = __builtin_amdgcn_permlane32_swap(hp[4 * k + 1], hp[4 * k + 3], false, false);
        st16(u32x4{r0[0], r1[0], r0[1], r1[1]}, wsr, hd + k * 512, 0);
      }
    }
  }
  {
    const int lane = lane_now();
    const int l15 = lane & 15, g4 = lane >> 4, q4 = l15 >> 2, p4 = lane & 3;
    const int tt = wv >> 1, kh = wv & 1;
    const int trow = pm * 64 + tt * 16 + l15;
    const u32 hoff = WS_H + (((4 * pm + tt) * 24 + 12 * kh) * 64 + lane) * 16;
    u32x4 hf[3][4];
    constexpr u32 A_T = G_SCR, LATE = RB + 16380;
    if (wv == 0) sts32(LATE, 0);
    u32x2 fa; u32 gtr;
    const u32 faa = WS_FA + (dn * 8 + (lane & 7)) * 8;
    auto a_tile = [&]() {
      glds1s(p.ws + WS_A + (dn * 80 + 8 * wv) * 4096 + pm * 128, (lane >> 3) * 4096 + (lane & 7) * 16, A_T + wv * 1024);
      if (wv < 2) glds1s(p.ws + WS_A + (dn * 80 + 64 + 8 * wv) * 4096 + pm * 128, (lane >> 3) * 4096 + (lane & 7) * 16, A_T + 8192 + wv * 1024);
    };
    const u32 oa = RB + lane * 16 + kh * 65536, ob = kh ? RB - 16384 + lane * 16 : RB + 65536 + lane * 16;
    u32x4 wf[6][5];
    auto ldw = [&](auto K) { constexpr int k = K; constexpr u32 cb = ((1 + k / 3) & 3) * 16384 + (k % 3) * 5120;
#pragma unroll
      for (int dtl = 0; dtl < 5; ++dtl) wf[k % 6][dtl] = lds128(cb + (k < 9 ? oa : ob) + dtl * 1024); };
    sfor<0, 6>(ldw);
    SB();
    if (idx < 24) {
      waitv<0>();
      bar();
      __builtin_amdgcn_s_sleep(H_PAD);
    } else {
      if (early) {
        const char* hw_ = p.ws + WS_H + ((4 * pm * 24 + 4 * ((lane & 7) < 6 ? (lane & 7) : 0)) * 64) * 16;
#pragma nounroll
        for (u32 sp = 0; sp < SPIN_CAP; ++sp) {
          if (__all(__hip_atomic_load((GAS u32*)hw_, RLX_AGENT) != POISON)) break;
          __builtin_amdgcn_s_sleep(3);
        }
      }
      bar0();
    }
    asm volatile("buffer_load_dwordx2 %0, %1, %2, 0 offen sc1" : "=v"(fa) : "v"(faa), "s"(wsr) : "memory");
    asm volatile("buffer_load_dword %0, %1, %2, 0 offen sc1" : "=v"(gtr) : "v"(WS_G + trow * 4), "s"(wsr) : "memory");
    sfor<0, 3>([&](auto J) { sfor<0, 4>([&](auto K) { ldz<K * 1024>(hf[J][K], hoff, wsr, J * 4096); }); if constexpr (J == 0) bar0(); if constexpr (J == 1) bar(); });
    bool got_a = false;
    f32x4 acc[5] = {};
    sfor<0, 12>([&](auto K) { constexpr int k = K;
      if constexpr ((k & 3) == 0) {
        constexpr int j = k / 4;
        waitz<4 * (2 - j)>(hf[j]);
        if (stale(hf[j])) {
          waitv<0>();
          u32 sp = 0;
#pragma nounroll
          do {
            __builtin_amdgcn_s_sleep(RETRY_SLEEP);
            sfor<0, 4>([&](auto KK) { ldzr<KK * 1024>(hf[j][KK], hoff, wsr, j * 4096); });
            waitz<0>(hf[j]);
          } while (++sp < SPIN_CAP && stale(hf[j]));
          sfor<j + 1, 3>([&](auto J) { sfor<0, 4>([&](auto KK) { ldzr<KK * 1024>(hf[J][KK], hoff, wsr, J * 4096); }); });
        }
        if constexpr (j == 0) {
          asm volatile("" : "+v"(fa), "+v"(gtr));
          got_a = __all(fa[0] == (u32)tag && fa[1] == (u32)(tag >> 32));
          if (got_a) a_tile();
        }
        if constexpr (j == 1) { if (!got_a) asm volatile("buffer_load_dwordx2 %0, %1, %2, 0 offen sc1" : "+v"(fa) : "v"(faa), "s"(wsr) : "memory"); }
        if constexpr (j == 2) {
          asm volatile("" : "+v"(fa));
          if (!got_a) { if (__all(fa[0] == (u32)tag && fa[1] == (u32)(tag >> 32))) a_tile(); else sts32(LATE, 1); }
        }
        SB();
      }
#pragma unroll
      for (int dtl = 0; dtl < 5; ++dtl) acc[dtl] = mfma16(wf[k % 6][dtl], hf[k / 4][k & 3], acc[dtl]);
      SB();
      if constexpr (k < 6) ldw(IC<k + 6>{});
      SB();
    });
    const u32 xa = (tt < 3 ? RB + tt * 5120 : G_SCR + 10240) + lane * 16;
    if (kh) { sts128(xa, asu4(acc[0])); sts128(xa + 1024, asu4(acc[1])); sts128(xa + 2048, asu4(acc[2])); }
    else { sts128(xa + 3072, asu4(acc[3])); sts128(xa + 4096, asu4(acc[4])); }
    waitv<0>();
    bar();
    if (__builtin_amdgcn_readfirstlane(*LP(const u32, LATE))) {
      if (wv == 0) poll(p.ws + faa, tag);
      bar();
      a_tile();
      waitv<0>();
      bar();
    }
    const float gt = asf(gtr);
    auto fin = [&](int k, f32x4 c) {
      const f32x4 o = c + asf4(lds128(xa + k * 1024));
      const u32x2 av = ldstr(A_T + (16 * k + 4 * g4 + q4) * 128 + tt * 32 + p4 * 8);
      const f32x2 lo = {o[0], o[1]}, hi = {o[2], o[3]};
      *(u32x2*)(p.out + trow * 640 + dn * 80 + k * 16 + 4 * g4) = u32x2{pk2(lo + gt * (bf2(av[0]) - lo)), pk2(hi + gt * (bf2(av[1]) - hi))};
    };
    if (kh) { fin(3, acc[3]); fin(4, acc[4]); } else { fin(0, acc[0]); fin(1, acc[1]); fin(2, acc[2]); }
    asm volatile("Lcode_tail:");
    if (blockIdx.x == 0 && wv == 1) {
      if (lane == 0) st_flag(p.ws + WS_EPOCH, epoch + 1);
    }
  }
}

extern "C" void kernel_launch(void* const* d_in, const int* in_sizes, int n_in, void* d_out, int out_size, void* d_ws, size_t ws_size, hipStream_t stream) {
  Params p;
  p.u = (const u16*)d_in[0]; p.Are = (const u16*)d_in[1]; p.Aim = (const u16*)d_in[2]; p.Bre = (const u16*)d_in[3]; p.Bim = (const u16*)d_in[4];
  p.Cre = (const u16*)d_in[5]; p.Cim = (const u16*)d_in[6]; p.ldt = (const u16*)d_in[7]; p.dskip = (const u16*)d_in[8];
  p.win = (const u16*)d_in[10]; p.wsp = (const u16*)d_in[11]; p.bsp = (const u16*)d_in[12]; p.wout = (const u16*)d_in[13]; p.gw = (const u16*)d_in[14];
  p.out = (u16*)d_out; p.ws = (char*)d_ws;
  (void)hipFuncSetAttribute((const void*)fused, hipFuncAttributeMaxDynamicSharedMemorySize, LDS_BYTES);
  fused<<<256, 512, LDS_BYTES, stream>>>(p);
}
```
